```python
import torch

def _softmax(x, axis=-1):
    return torch.softmax(x, dim=axis)
def _gelu(x):
    return torch.nn.functional.gelu(x, approximate="tanh")
def _silu(x):
    return torch.nn.functional.silu(x)
def _sigmoid(x):
    return torch.sigmoid(x)
def _layernorm(x, axis=-1, eps=1e-5):
    m = x.mean(dim=axis, keepdim=True)
    v = x.var(dim=axis, keepdim=True, unbiased=False)
    return (x - m) / torch.sqrt(v + eps)
def _scatter_add(tgt, idx, src, axis=0):
    return tgt.clone().index_add_(axis, idx, src)
def _segment_sum(src, seg, n):
    out = torch.zeros((n,) + src.shape[1:], dtype=src.dtype)
    return out.index_add_(0, seg, src)
def _pad(x, pw):
    flat = tuple(v for pair in reversed(pw) for v in pair)
    return torch.nn.functional.pad(x, flat)

kernel_name = 'standalone__adm_resblock__diffusion'

def setup_inputs():

    _w = (lambda a: (lambda t: t.to(torch.bfloat16) if t.is_floating_point() else t)(torch.as_tensor(a)))
    B, C, HW, E = 2, 320, 64, 1152
    sc = (9.0 * C) ** -0.5
    return {
        "x": _w((lambda *s: torch.randn(*s, dtype=torch.bfloat16) * 0.05)(B, C, HW, HW)),
        "t_emb": _w((lambda *s: torch.randn(*s, dtype=torch.bfloat16) * 0.2)(B, E)),
        "g1": _w(1.0 + 0.1 * (lambda *s: torch.randn(*s, dtype=torch.bfloat16) * 0.2)(C)),
        "be1": _w(0.1 * (lambda *s: torch.randn(*s, dtype=torch.bfloat16) * 0.2)(C)),
        "w1": _w((lambda *s: torch.randn(*s, dtype=torch.bfloat16) * 0.2)(C, C, 3, 3) * sc),
        "w_emb": _w((lambda *s: torch.randn(*s, dtype=torch.bfloat16) * 0.2)(E, C) * (E ** -0.5)),
        "g2": _w(1.0 + 0.1 * (lambda *s: torch.randn(*s, dtype=torch.bfloat16) * 0.2)(C)),
        "be2": _w(0.1 * (lambda *s: torch.randn(*s, dtype=torch.bfloat16) * 0.2)(C)),
        "w2": _w((lambda *s: torch.randn(*s, dtype=torch.bfloat16) * 0.2)(C, C, 3, 3) * sc),
    }

def reference(x, t_emb, g1, be1, w1, w_emb, g2, be2, w2):
    B, C, H, W = x.shape
    G, eps = 4, 1e-5
    def gn(z, gamma, beta):
        zr = z.reshape(B, G, (C // G) * H * W)
        mu = zr.mean(-1).reshape(B, G, 1)
        var = ((zr - mu) ** 2).mean(-1).reshape(B, G, 1)
        zn = ((zr - mu) / torch.sqrt(var + eps)).reshape(B, C, H, W)
        return zn * gamma.reshape(1, C, 1, 1) + beta.reshape(1, C, 1, 1)
    def conv3(z, w):
        zp = _pad(z, ((0, 0), (0, 0), (1, 1), (1, 1)))
        taps = [torch.einsum("bchw,oc->bohw", zp[:, :, i:i+H, j:j+W], w[:, :, i, j]) for i in range(3) for j in range(3)]
        out = taps[0]
        for t in taps[1:]:
            out = out + t
        return out
    h = conv3(_silu(gn(x, g1, be1)), w1)
    emb = _silu(t_emb) @ w_emb
    h = h + emb.reshape(B, C, 1, 1)
    h = conv3(_silu(gn(h, g2, be2)), w2)
    return x + h
```

```cpp
#include <hip/hip_runtime.h>
#include <hip/hip_bf16.h>

typedef unsigned u32;
typedef unsigned long long u64;
typedef unsigned short u16;
typedef __attribute__((ext_vector_type(4))) unsigned u32x4;
typedef __attribute__((ext_vector_type(2))) unsigned u32x2;
typedef __attribute__((ext_vector_type(4))) float f32x4;
typedef __attribute__((ext_vector_type(2))) float f32x2;
typedef __attribute__((ext_vector_type(8))) __bf16 bf16x8;
typedef __attribute__((ext_vector_type(2))) __bf16 bf16x2;
#define GAS __attribute__((address_space(1)))
typedef GAS u32 gu32;
typedef GAS u64 gu64;
#define RLX_AGENT __ATOMIC_RELAXED, __HIP_MEMORY_SCOPE_AGENT

constexpr size_t ACT_BYTES = 5242880;
constexpr size_t WP_BYTES = 1884160;
constexpr size_t OFF_ACT1 = 0;
constexpr size_t OFF_ACT2 = ACT_BYTES;
constexpr size_t OFF_WP1 = 2 * ACT_BYTES;
constexpr size_t OFF_WP2 = OFF_WP1 + WP_BYTES;
constexpr size_t OFF_EMB = OFF_WP2 + WP_BYTES;
constexpr size_t OFF_CTL = OFF_EMB + 40960;
constexpr size_t OFF_G1 = OFF_CTL + 1024;
constexpr size_t OFF_G2 = OFF_G1 + 4096;
constexpr float LOG2E = 1.4426950408889634f;
constexpr float EPS = 1e-5f;
constexpr u32 SPIN_CAP = 1u << 16;

__device__ __forceinline__ float bf_lo(u32 v) { return __builtin_bit_cast(float, v << 16); }
__device__ __forceinline__ float bf_hi(u32 v) { return __builtin_bit_cast(float, v & 0xffff0000u); }
__device__ __forceinline__ u32 pack_bf16(float lo, float hi) {
  f32x2 v = {lo, hi};
  return __builtin_bit_cast(u32, __builtin_convertvector(v, bf16x2));
}
__device__ __forceinline__ float silu_l2(float yl) {
  float e = __builtin_amdgcn_exp2f(-yl);
  return yl * __builtin_amdgcn_rcpf(__builtin_fmaf(e, LOG2E, LOG2E));
}
template <int CTRL, int RM>
__device__ __forceinline__ float dpp_add(float v) {
  int t = __builtin_amdgcn_update_dpp(0, __builtin_bit_cast(int, v), CTRL, RM, 0xf, true);
  return v + __builtin_bit_cast(float, t);
}
__device__ __forceinline__ float rdlane(float v, int l) {
  return __builtin_bit_cast(float, __builtin_amdgcn_readlane(__builtin_bit_cast(int, v), l));
}
__device__ __forceinline__ float wave_sum(float v) {
  v = dpp_add<0x111, 0xf>(v); v = dpp_add<0x112, 0xf>(v); v = dpp_add<0x114, 0xf>(v); v = dpp_add<0x118, 0xf>(v);
  v = dpp_add<0x142, 0xa>(v); v = dpp_add<0x143, 0xc>(v);
  return rdlane(v, 63);
}
__device__ __forceinline__ float row0_sum(float v) {
  v = dpp_add<0x111, 0xf>(v); v = dpp_add<0x112, 0xf>(v); v = dpp_add<0x114, 0xf>(v); v = dpp_add<0x118, 0xf>(v);
  return rdlane(v, 15);
}
__device__ __forceinline__ float row_allsum(float v) {
  v = dpp_add<0x128, 0xf>(v); v = dpp_add<0x124, 0xf>(v); v = dpp_add<0x122, 0xf>(v); v = dpp_add<0x121, 0xf>(v);
  return v;
}

__device__ __forceinline__ void exchange_stats(gu64* gran, int member, u32 tag, float mean, float m2, int lane,
                                               float& gmean, float& rstd) {
  if (lane < 2) {
    float v = lane ? m2 : mean;
    __hip_atomic_store(gran + 2 * member + lane, ((u64)tag << 32) | __builtin_bit_cast(u32, v), RLX_AGENT);
  }
  u64 x;
  for (u32 spins = 0;; ++spins) {
    x = __hip_atomic_load(gran + lane, RLX_AGENT);
    if (__all((u32)(x >> 32) == tag) || spins > SPIN_CAP) break;
    __builtin_amdgcn_s_sleep(1);
  }
  float v = __builtin_bit_cast(float, (u32)x);
  bool odd = lane & 1;
  gmean = wave_sum(odd ? 0.f : v) * (1.f / 32.f);
  float dd = v - gmean;
  float tot = wave_sum(odd ? v : 10240.f * dd * dd);
  float var = fmaxf(tot * (1.f / 327680.f), 0.f);
  rstd = 1.0f / __builtin_sqrtf(var + EPS);
}

__device__ __forceinline__ int chan1(int n, int i) {
  return n < 4 ? 32 * (n >> 1) + 8 * (i >> 2) + 4 * (n & 1) + (i & 3) : 64 + i;
}

__global__ __launch_bounds__(768) void k_prep(const u32* __restrict__ x, const u16* __restrict__ temb,
                                              const u32* __restrict__ g1, const u32* __restrict__ be1,
                                              const char* __restrict__ w1, const char* __restrict__ wemb,
                                              const char* __restrict__ w2, char* __restrict__ ws) {
  __shared__ float sm[32];
  const int lane = threadIdx.x & 63;
  const int wv = __builtin_amdgcn_readfirstlane(threadIdx.x >> 6);
  const int bid = blockIdx.x;
  if (wv < 10) {
    const int xb = bid & 7, idx = bid >> 3;
    const int b = xb >> 2, band = xb & 3, g = idx & 3, rp = idx >> 2;
    const int gi = b * 4 + g, member = band * 8 + rp;
    const int y0 = band * 16 + rp * 2, c8 = g * 10 + wv;
    const u32* src = x + (size_t)(b * 320 + c8 * 8) * 2048 + y0 * 32 + lane;
    u32 v[8];
#pragma unroll
    for (int e = 0; e < 8; e++) v[e] = src[e * 2048];
    gu32* ep = (gu32*)(ws + OFF_CTL + gi * 128);
    u32 epoch = 0;
    if (wv == 0) epoch = __hip_atomic_load(ep, RLX_AGENT);
    u32 gw[4], bw[4];
#pragma unroll
    for (int e = 0; e < 4; e++) { gw[e] = g1[c8 * 4 + e]; bw[e] = be1[c8 * 4 + e]; }
    const float K = __builtin_bit_cast(float, __builtin_amdgcn_readfirstlane(v[0] << 16));
    float d[16];
    float s = 0.f, q = 0.f;
#pragma unroll
    for (int e = 0; e < 8; e++) {
      d[e] = bf_lo(v[e]) - K; d[8 + e] = bf_hi(v[e]) - K;
      s += d[e]; q = __builtin_fmaf(d[e], d[e], q);
      s += d[8 + e]; q = __builtin_fmaf(d[8 + e], d[8 + e], q);
    }
    s = wave_sum(s); q = wave_sum(q);
    if (lane == 0) { sm[2 * wv] = K + s * (1.f / 1024.f); sm[2 * wv + 1] = q - s * s * (1.f / 1024.f); }
    __syncthreads();
    if (wv == 0) {
      float mv = lane < 10 ? sm[2 * lane] : 0.f, m2 = lane < 10 ? sm[2 * lane + 1] : 0.f;
      float mean = row0_sum(mv) * 0.1f;
      float dd = lane < 10 ? mv - mean : 0.f;
      float M2 = row0_sum(m2 + 1024.f * dd * dd);
      const u32 tag = epoch + 0x9E3779B9u;
      float gm, rs;
      exchange_stats((gu64*)(ws + OFF_G1) + gi * 64, member, tag, mean, M2, lane, gm, rs);
      if (lane == 0) {
        sm[24] = gm; sm[25] = rs;
        if (member == 0) __hip_atomic_store(ep, tag, RLX_AGENT);
      }
    }
    __syncthreads();
    const float mu = sm[24] - K, rs = sm[25] * LOG2E;
    float o[16];
#pragma unroll
    for (int e = 0; e < 8; e++) {
      float ga = (e & 1) ? bf_hi(gw[e >> 1]) : bf_lo(gw[e >> 1]);
      float be = (e & 1) ? bf_hi(bw[e >> 1]) : bf_lo(bw[e >> 1]);
      float a = rs * ga, c = __builtin_fmaf(-mu, a, be * LOG2E);
      o[e] = silu_l2(__builtin_fmaf(d[e], a, c));
      o[8 + e] = silu_l2(__builtin_fmaf(d[8 + e], a, c));
    }
    u32x4 p0, p1;
#pragma unroll
    for (int e = 0; e < 4; e++) { p0[e] = pack_bf16(o[2 * e], o[2 * e + 1]); p1[e] = pack_bf16(o[8 + 2 * e], o[9 + 2 * e]); }
    u32x4* dst = (u32x4*)(ws + OFF_ACT1) + (size_t)(b * 40 + c8) * 4096 + y0 * 64 + lane * 2;
    dst[0] = p0; dst[1] = p1;
  } else {
    const int a = bid * 2 + (wv - 10);
    if (a < 400) {
      const int cv = a >= 200, r = a - cv * 200;
      const int tile = r / 10, cg = r - tile * 10;
      const int og = tile / 5, n = tile - og * 5;
      const int i = lane & 15, c8 = cg * 4 + (lane >> 4);
      const int o = og * 80 + (cv ? n * 16 + i : chan1(n, i));
      const u32x4* src = (const u32x4*)((cv ? w2 : w1) + (size_t)o * 5760 + c8 * 144);
      u32 t[36];
#pragma unroll
      for (int k = 0; k < 9; k++) { u32x4 u = src[k]; t[4 * k] = u[0]; t[4 * k + 1] = u[1]; t[4 * k + 2] = u[2]; t[4 * k + 3] = u[3]; }
      __builtin_amdgcn_s_barrier();
      const int ks = c8 / 10, cl = c8 - ks * 10;
      const int q0 = 18 * (cl >> 1) + (cl & 1);
      u32x4* dbase = (u32x4*)(ws + (cv ? OFF_WP2 : OFF_WP1)) + ((og * 4 + ks) * 115 + n) * 64 + i;
#pragma unroll
      for (int tp = 0; tp < 9; tp++) {
        u32x4 f;
#pragma unroll
        for (int dd = 0; dd < 4; dd++) {
          const int e0 = 18 * dd + tp, e1 = e0 + 9;
          const u32 sel = ((e0 & 1) ? 0x0302u : 0x0100u) | ((e1 & 1) ? 0x07060000u : 0x05040000u);
          f[dd] = __builtin_amdgcn_perm(t[e1 >> 1], t[e0 >> 1], sel);
        }
        const int q = q0 + 2 * tp;
        dbase[(q >> 2) * 320 + (q & 3) * 16] = f;
      }
    } else if (a < 480) {
      const int r = a - 400, sl = r / 5, cg = r - sl * 5;
      const int e0 = sl * 72 + (lane >> 3);
      const u32x4* src = (const u32x4*)(wemb + (size_t)e0 * 640 + cg * 128 + (lane & 7) * 16);
      u32x4 u[9];
      u32 t0[9], t1[9];
#pragma unroll
      for (int k = 0; k < 9; k++) { u[k] = src[k * 320]; t0[k] = temb[e0 + 8 * k]; t1[k] = temb[1152 + e0 + 8 * k]; }
      __builtin_amdgcn_s_barrier();
      float a0[8], a1[8];
#pragma unroll
      for (int c = 0; c < 8; c++) { a0[c] = 0.f; a1[c] = 0.f; }
#pragma unroll
      for (int k = 0; k < 9; k++) {
        float s0 = silu_l2(bf_lo(t0[k]) * LOG2E), s1 = silu_l2(bf_lo(t1[k]) * LOG2E);
#pragma unroll
        for (int c = 0; c < 8; c++) {
          float wv_ = (c & 1) ? bf_hi(u[k][c >> 1]) : bf_lo(u[k][c >> 1]);
          a0[c] = __builtin_fmaf(s0, wv_, a0[c]); a1[c] = __builtin_fmaf(s1, wv_, a1[c]);
        }
      }
#pragma unroll
      for (int c = 0; c < 8; c++) {
        a0[c] = dpp_add<0x128, 0xf>(a0[c]); a1[c] = dpp_add<0x128, 0xf>(a1[c]);
        a0[c] += __shfl_xor(a0[c], 16); a1[c] += __shfl_xor(a1[c], 16);
        a0[c] += __shfl_xor(a0[c], 32); a1[c] += __shfl_xor(a1[c], 32);
      }
      if (lane < 8) {
        f32x4* d0 = (f32x4*)(ws + OFF_EMB) + (sl * 320 + cg * 64 + lane * 8) / 4;
        d0[0] = f32x4{a0[0], a0[1], a0[2], a0[3]}; d0[1] = f32x4{a0[4], a0[5], a0[6], a0[7]};
        d0[1280] = f32x4{a1[0], a1[1], a1[2], a1[3]}; d0[1281] = f32x4{a1[4], a1[5], a1[6], a1[7]};
      }
    } else {
      const int r = a - 480;
      u32x4* dbase = (u32x4*)(ws + ((r >> 4) ? OFF_WP2 : OFF_WP1)) + ((r & 15) * 115 + 110) * 64 + 32;
      const u32x4 z = {0u, 0u, 0u, 0u};
      if (lane < 32) {
#pragma unroll
        for (int n = 0; n < 5; n++) dbase[n * 64 + lane] = z;
      }
      if (r < 8) ((u64*)(ws + OFF_G2))[r * 64 + lane] = 0ull;
    }
  }
}

template <int I> struct IC { static constexpr int value = I; };
template <int I, int N, class F>
__device__ __forceinline__ void static_for_(F&& f) {
  if constexpr (I < N) { f(IC<I>{}); static_for_<I + 1, N>(f); }
}
template <int N, class F>
__device__ __forceinline__ void static_for(F&& f) { static_for_<0, N>(f); }

constexpr int REG = 30720;
__host__ __device__ constexpr int a_off(int P) { return (P / 9) * 6144 + ((P % 9) / 3) * 288 + (P % 3) * 16; }
__host__ __device__ constexpr int a_dk(int s) { return s == 22 ? 0 : ((2 * s) % 9 == 8 ? 2 : ((2 * s) % 3 == 2 ? 1 : 0)); }

template <int CONV>
__global__ __launch_bounds__(256, 1) void k_conv(const char* __restrict__ act, const char* __restrict__ wp,
                                                 const char* __restrict__ p0, const char* __restrict__ p1,
                                                 char* __restrict__ ws, char* __restrict__ out) {
  __shared__ __attribute__((aligned(16))) char lds[4 * REG + 256];
  const int lane = threadIdx.x & 63;
  const int w = __builtin_amdgcn_readfirstlane(threadIdx.x >> 6);
  const int bid = blockIdx.x;
  const int xb = bid & 7, idx = bid >> 3;
  const int b = xb >> 2, band = xb & 3, og = idx & 3, tile = idx >> 2;
  const int y0 = band * 16 + (tile >> 2) * 8, x0 = (tile & 3) * 16;
  const int li = lane & 15, lj = lane >> 4;

  const __amdgpu_buffer_rsrc_t rsA =
      __builtin_amdgcn_make_buffer_rsrc((void*)(act + (size_t)(b * 40 + w * 10) * 65536), 0, 10 * 65536, 0x00020000);
  const __amdgpu_buffer_rsrc_t rsB =
      __builtin_amdgcn_make_buffer_rsrc((void*)(wp + (size_t)(og * 4 + w) * 117760), 0, 117760, 0x00020000);
  u32 voffA[3];
#pragma unroll
  for (int i = 0; i < 3; i++) {
    const int p = i * 64 + lane, row = p / 18, px = p - row * 18;
    const int yy = y0 - 1 + row, xx = x0 - 1 + px;
    const bool ok = p < 180 && (u32)yy < 64u && (u32)xx < 64u;
    voffA[i] = ok ? (u32)(yy * 64 + xx) * 16u : 0x80000000u;
  }
  const u32 voffB = lane * 16;
  char* const ldsw = lds + w * REG + lane * 16;

  u32x4 st[2][6];
  u32x4 bf[4][5];
  u32x4 af[2][8];
#define LOAD_A(c, slot)                                                                         \
  _Pragma("unroll") for (int i_ = 0; i_ < 6; i_++) st[slot][i_] =                               \
      __builtin_amdgcn_raw_buffer_load_b128(rsA, voffA[i_ % 3], ((c) * 2 + i_ / 3) * 65536, 0);
#define WRITE_A(c, slot)                                                                        \
  _Pragma("unroll") for (int i_ = 0; i_ < 6; i_++) *(u32x4*)(ldsw + (c) * 6144 + i_ * 1024) = st[slot][i_];
#define LOAD_B(s, n) bf[(s) & 3][n] = __builtin_amdgcn_raw_buffer_load_b128(rsB, voffB, ((s) * 5 + (n)) * 1024, 0);

  LOAD_A(0, 0)
#pragma unroll
  for (int n = 0; n < 5; n++) { LOAD_B(0, n) }
#pragma unroll
  for (int n = 0; n < 5; n++) { LOAD_B(1, n) }
  LOAD_A(1, 1)
#pragma unroll
  for (int n = 0; n < 5; n++) { LOAD_B(2, n) }
#pragma unroll
  for (int n = 0; n < 5; n++) { LOAD_B(3, n) }

  f32x4 em[5];
  u32x4 gq[2], bq[2];
  u32x2 g4, b4;
  u32x2 xr[2][5];
  if (CONV == 1) {
    const float* ep = (const float*)(ws + OFF_EMB) + (b * 16 + li) * 320 + og * 80;
#pragma unroll
    for (int n = 0; n < 5; n++) em[n] = *(const f32x4*)(ep + chan1(n, 4 * lj));
#pragma unroll
    for (int t = 0; t < 2; t++) {
      gq[t] = *(const u32x4*)(p0 + (og * 80 + 32 * t + 8 * lj) * 2);
      bq[t] = *(const u32x4*)(p1 + (og * 80 + 32 * t + 8 * lj) * 2);
    }
    g4 = *(const u32x2*)(p0 + (og * 80 + 64 + 4 * lj) * 2);
    b4 = *(const u32x2*)(p1 + (og * 80 + 64 + 4 * lj) * 2);
  }

  const char* abase[4][3];
  {
    const char* lb = lds + w * REG + (lj & 1) * 3072 + li * 16;
    const bool hi = lj >> 1;
#pragma unroll
    for (int p = 0; p < 4; p++) {
      const char* r = lb + ((p + w) & 3) * 576;
      abase[p][0] = r + (hi ? 16 : 0); abase[p][1] = r + (hi ? 256 : 0); abase[p][2] = r + (hi ? 5536 : 0);
    }
  }
#define READ_A(s, m) af[(s) & 1][m] = *(const u32x4*)(abase[(m) >> 1][a_dk(s)] + a_off(2 * (s)) + ((m) & 1) * 288);

  f32x4 acc[8][5];
#pragma unroll
  for (int m = 0; m < 8; m++)
#pragma unroll
    for (int n = 0; n < 5; n++) acc[m][n] = f32x4{0.f, 0.f, 0.f, 0.f};

  WRITE_A(0, 0)
#pragma unroll
  for (int m = 0; m < 8; m++) { READ_A(0, m) }
  __builtin_amdgcn_sched_barrier(0);

  static_for<23>([&](auto sc) __attribute__((always_inline)) {
    constexpr int s = decltype(sc)::value;
#pragma unroll
    for (int n = 0; n < 5; n++) {
#pragma unroll
      for (int m = 0; m < 8; m++) {
        const bf16x8 xa = __builtin_bit_cast(bf16x8, af[s & 1][m]);
        const bf16x8 wb = __builtin_bit_cast(bf16x8, bf[s & 3][n]);
        if (CONV == 1) acc[m][n] = __builtin_amdgcn_mfma_f32_16x16x32_bf16(wb, xa, acc[m][n], 0, 0, 0);
        else acc[m][n] = __builtin_amdgcn_mfma_f32_16x16x32_bf16(xa, wb, acc[m][n], 0, 0, 0);
        if (s < 22 && n < 4 && (m == 1 || m == 5)) { READ_A(s + 1, 2 * n + (m >> 2)) __builtin_amdgcn_sched_barrier(0); }
        if (n == 4 && m < 6) {
          if (s == 1) { *(u32x4*)(ldsw + 1 * 6144 + m * 1024) = st[1][m]; }
          if (s == 6) { *(u32x4*)(ldsw + 2 * 6144 + m * 1024) = st[0][m]; }
          if (s == 10) { *(u32x4*)(ldsw + 3 * 6144 + m * 1024) = st[1][m]; }
          if (s == 15) { *(u32x4*)(ldsw + 4 * 6144 + m * 1024) = st[0][m]; }
          if (s == 0) st[0][m] = __builtin_amdgcn_raw_buffer_load_b128(rsA, voffA[m % 3], (4 + m / 3) * 65536, 0);
          if (s == 3) st[1][m] = __builtin_amdgcn_raw_buffer_load_b128(rsA, voffA[m % 3], (6 + m / 3) * 65536, 0);
          if (s == 7) st[0][m] = __builtin_amdgcn_raw_buffer_load_b128(rsA, voffA[m % 3], (8 + m / 3) * 65536, 0);
          if (CONV == 2 && (s == 18 || s == 19) && m < 5) {
            const int r = s & 1;
            xr[r][m] = *(const u32x2*)(p0 + ((size_t)(b * 320 + og * 80 + m * 16 + li) * 4096 + (y0 + 2 * w + r) * 64 + x0 + 4 * lj) * 2);
          }
          __builtin_amdgcn_sched_barrier(0);
        }
        if (CONV == 1 && s == 5 && m < 4) { em[n][m] = row_allsum(em[n][m]); }
      }
      if (s + 4 < 23) { LOAD_B(s + 4, n) }
      __builtin_amdgcn_sched_barrier(0);
    }
  });

#pragma unroll
  for (int m = 2; m < 8; m++)
#pragma unroll
    for (int n = 0; n < 5; n++) *(f32x4*)(ldsw + ((m - 2) * 5 + n) * 1024) = acc[m][n];
  __syncthreads();
#pragma unroll
  for (int d = 1; d < 4; d++) {
    const char* src = lds + ((w - d) & 3) * REG + (d - 1) * 10240 + lane * 16;
#pragma unroll
    for (int r = 0; r < 2; r++)
#pragma unroll
      for (int n = 0; n < 5; n++) acc[r][n] += *(const f32x4*)(src + (r * 5 + n) * 1024);
  }

  if (CONV == 1) {
    float* sm = (float*)(lds + 4 * REG);
    const float K = __builtin_bit_cast(float, __builtin_amdgcn_readfirstlane(__builtin_bit_cast(int, acc[0][0][0] + em[0][0])));
    float s = 0.f, q = 0.f;
#pragma unroll
    for (int n = 0; n < 5; n++)
#pragma unroll
      for (int k = 0; k < 4; k++) {
        const float ek = em[n][k] - K;
#pragma unroll
        for (int r = 0; r < 2; r++) {
          const float dv = acc[r][n][k] + ek;
          acc[r][n][k] = dv; s += dv; q = __builtin_fmaf(dv, dv, q);
        }
      }
    s = wave_sum(s); q = wave_sum(q);
    if (lane == 0) { sm[2 * w] = K + s * (1.f / 2560.f); sm[2 * w + 1] = q - s * s * (1.f / 2560.f); }
    __syncthreads();
    if (w == 0) {
      float mv = lane < 4 ? sm[2 * lane] : 0.f, m2 = lane < 4 ? sm[2 * lane + 1] : 0.f;
      float mean = row0_sum(mv) * 0.25f;
      float dd = lane < 4 ? mv - mean : 0.f;
      float M2 = row0_sum(m2 + 2560.f * dd * dd);
      float gm, rs;
      exchange_stats((gu64*)(ws + OFF_G2) + (b * 4 + og) * 64, band * 8 + tile, 1u, mean, M2, lane, gm, rs);
      if (lane == 0) { sm[16] = gm; sm[17] = rs; }
    }
    float ga[5][4], be[5][4];
#pragma unroll
    for (int n = 0; n < 5; n++)
#pragma unroll
      for (int k = 0; k < 4; k++) {
        const u32 gw = n < 4 ? gq[n >> 1][(n & 1) * 2 + (k >> 1)] : g4[k >> 1];
        const u32 bw = n < 4 ? bq[n >> 1][(n & 1) * 2 + (k >> 1)] : b4[k >> 1];
        ga[n][k] = ((k & 1) ? bf_hi(gw) : bf_lo(gw)) * LOG2E;
        be[n][k] = ((k & 1) ? bf_hi(bw) : bf_lo(bw)) * LOG2E;
        acc[0][n][k] *= ga[n][k]; acc[1][n][k] *= ga[n][k];
      }
    __syncthreads();
    const float rs = sm[17], mr = (sm[16] - K) * rs;
    u32 pk[2][5][2];
#pragma unroll
    for (int n = 0; n < 5; n++) {
      float o[2][4];
#pragma unroll
      for (int k = 0; k < 4; k++) {
        const float c = __builtin_fmaf(-mr, ga[n][k], be[n][k]);
        o[0][k] = silu_l2(__builtin_fmaf(acc[0][n][k], rs, c));
        o[1][k] = silu_l2(__builtin_fmaf(acc[1][n][k], rs, c));
      }
#pragma unroll
      for (int r = 0; r < 2; r++) { pk[r][n][0] = pack_bf16(o[r][0], o[r][1]); pk[r][n][1] = pack_bf16(o[r][2], o[r][3]); }
    }
#pragma unroll
    for (int r = 0; r < 2; r++) {
      char* dst = out + ((size_t)(b * 40 + og * 10) * 4096 + (y0 + 2 * w + r) * 64 + x0 + li) * 16;
#pragma unroll
      for (int t = 0; t < 2; t++)
        *(u32x4*)(dst + (size_t)(4 * t + lj) * 65536) = u32x4{pk[r][2 * t][0], pk[r][2 * t][1], pk[r][2 * t + 1][0], pk[r][2 * t + 1][1]};
      *(u32x2*)(dst + (size_t)(8 + (lj >> 1)) * 65536 + (lj & 1) * 8) = u32x2{pk[r][4][0], pk[r][4][1]};
    }
  } else {
#pragma unroll
    for (int r = 0; r < 2; r++)
#pragma unroll
      for (int n = 0; n < 5; n++) {
        u32x2 o;
        o[0] = pack_bf16(acc[r][n][0] + bf_lo(xr[r][n][0]), acc[r][n][1] + bf_hi(xr[r][n][0]));
        o[1] = pack_bf16(acc[r][n][2] + bf_lo(xr[r][n][1]), acc[r][n][3] + bf_hi(xr[r][n][1]));
        *(u32x2*)(out + ((size_t)(b * 320 + og * 80 + n * 16 + li) * 4096 + (y0 + 2 * w + r) * 64 + x0 + 4 * lj) * 2) = o;
      }
  }
}

extern "C" void kernel_launch(void* const* d_in, const int* in_sizes, int n_in, void* d_out, int out_size,
                              void* d_ws, size_t ws_size, hipStream_t stream) {
  char* ws = (char*)d_ws;
  k_prep<<<256, 768, 0, stream>>>((const u32*)d_in[0], (const u16*)d_in[1], (const u32*)d_in[2], (const u32*)d_in[3],
                                  (const char*)d_in[4], (const char*)d_in[5], (const char*)d_in[8], ws);
  k_conv<1><<<256, 256, 0, stream>>>(ws + OFF_ACT1, ws + OFF_WP1, (const char*)d_in[6], (const char*)d_in[7], ws,
                                     ws + OFF_ACT2);
  k_conv<2><<<256, 256, 0, stream>>>(ws + OFF_ACT2, ws + OFF_WP2, (const char*)d_in[0], (const char*)d_in[0], ws,
                                     (char*)d_out);
}
```

```cpp
#include <hip/hip_runtime.h>
#include <hip/hip_bf16.h>

typedef unsigned u32;
typedef unsigned long long u64;
typedef unsigned short u16;
typedef __attribute__((ext_vector_type(4))) unsigned u32x4;
typedef __attribute__((ext_vector_type(2))) unsigned u32x2;
typedef __attribute__((ext_vector_type(4))) float f32x4;
typedef __attribute__((ext_vector_type(2))) float f32x2;
typedef __attribute__((ext_vector_type(8))) __bf16 bf16x8;
typedef __attribute__((ext_vector_type(2))) __bf16 bf16x2;
#define GAS __attribute__((address_space(1)))
typedef GAS u32 gu32;
typedef GAS u64 gu64;
#define RLX_AGENT __ATOMIC_RELAXED, __HIP_MEMORY_SCOPE_AGENT

constexpr size_t ACT_BYTES = 5242880;
constexpr size_t WP_BYTES = 1884160;
constexpr size_t OFF_ACT1 = 0;
constexpr size_t OFF_ACT2 = ACT_BYTES;
constexpr size_t OFF_WP1 = 2 * ACT_BYTES;
constexpr size_t OFF_WP2 = OFF_WP1 + WP_BYTES;
constexpr size_t OFF_EMB = OFF_WP2 + WP_BYTES;
constexpr size_t OFF_CTL = OFF_EMB + 40960;
constexpr size_t OFF_G1 = OFF_CTL + 1024;
constexpr size_t OFF_G2 = OFF_G1 + 4096;
#define STAMP_DECL
#define STAMP(i) {}
#define STAMP_OUT(k, wv)
constexpr float LOG2E = 1.4426950408889634f;
constexpr float EPS = 1e-5f;
constexpr u32 SPIN_CAP = 1u << 16;

__device__ __forceinline__ float bf_lo(u32 v) { return __builtin_bit_cast(float, v << 16); }
__device__ __forceinline__ float bf_hi(u32 v) { return __builtin_bit_cast(float, v & 0xffff0000u); }
__device__ __forceinline__ u32 pack_bf16(float lo, float hi) {
  f32x2 v = {lo, hi};
  return __builtin_bit_cast(u32, __builtin_convertvector(v, bf16x2));
}
__device__ __forceinline__ float silu_l2(float yl) {
  float e = __builtin_amdgcn_exp2f(-yl);
  return yl * __builtin_amdgcn_rcpf(__builtin_fmaf(e, LOG2E, LOG2E));
}
template <int CTRL, int RM>
__device__ __forceinline__ float dpp_add(float v) {
  int t = __builtin_amdgcn_update_dpp(0, __builtin_bit_cast(int, v), CTRL, RM, 0xf, true);
  return v + __builtin_bit_cast(float, t);
}
__device__ __forceinline__ float rdlane(float v, int l) {
  return __builtin_bit_cast(float, __builtin_amdgcn_readlane(__builtin_bit_cast(int, v), l));
}
__device__ __forceinline__ float wave_sum(float v) {
  v = dpp_add<0x111, 0xf>(v); v = dpp_add<0x112, 0xf>(v); v = dpp_add<0x114, 0xf>(v); v = dpp_add<0x118, 0xf>(v);
  v = dpp_add<0x142, 0xa>(v); v = dpp_add<0x143, 0xc>(v);
  return rdlane(v, 63);
}
__device__ __forceinline__ float row0_sum(float v) {
  v = dpp_add<0x111, 0xf>(v); v = dpp_add<0x112, 0xf>(v); v = dpp_add<0x114, 0xf>(v); v = dpp_add<0x118, 0xf>(v);
  return rdlane(v, 15);
}
__device__ __forceinline__ float row_allsum(float v) {
  v = dpp_add<0x128, 0xf>(v); v = dpp_add<0x124, 0xf>(v); v = dpp_add<0x122, 0xf>(v); v = dpp_add<0x121, 0xf>(v);
  return v;
}

__device__ __forceinline__ void exchange_stats(gu64* gran, int member, u32 tag, float mean, float m2, int lane,
                                               float& gmean, float& rstd) {
  if (lane < 2) {
    float v = lane ? m2 : mean;
    __hip_atomic_store(gran + 2 * member + lane, ((u64)tag << 32) | __builtin_bit_cast(u32, v), RLX_AGENT);
  }
  u64 x;
  for (u32 spins = 0;; ++spins) {
    x = __hip_atomic_load(gran + lane, RLX_AGENT);
    if (__all((u32)(x >> 32) == tag) || spins > SPIN_CAP) break;
    __builtin_amdgcn_s_sleep(1);
  }
  float v = __builtin_bit_cast(float, (u32)x);
  bool odd = lane & 1;
  gmean = wave_sum(odd ? 0.f : v) * (1.f / 32.f);
  float dd = v - gmean;
  float tot = wave_sum(odd ? v : 10240.f * dd * dd);
  float var = fmaxf(tot * (1.f / 327680.f), 0.f);
  rstd = 1.0f / __builtin_sqrtf(var + EPS);
}

__device__ __forceinline__ int chan1(int n, int i) {
  return n < 4 ? 32 * (n >> 1) + 8 * (i >> 2) + 4 * (n & 1) + (i & 3) : 64 + i;
}

__global__ __launch_bounds__(768) void k_prep(const u32* __restrict__ x, const u16* __restrict__ temb,
                                              const u32* __restrict__ g1, const u32* __restrict__ be1,
                                              const char* __restrict__ w1, const char* __restrict__ wemb,
                                              const char* __restrict__ w2, char* __restrict__ ws) {
  __shared__ float sm[32];
  const int lane = threadIdx.x & 63;
  const int wv = __builtin_amdgcn_readfirstlane(threadIdx.x >> 6);
  const int bid = blockIdx.x;
  STAMP_DECL
  STAMP(0)
  if (wv < 10) {
    const int xb = bid & 7, idx = bid >> 3;
    const int b = xb >> 2, band = xb & 3, g = idx & 3, rp = idx >> 2;
    const int gi = b * 4 + g, member = band * 8 + rp;
    const int y0 = band * 16 + rp * 2, c8 = g * 10 + wv;
    const u32* src = x + (size_t)(b * 320 + c8 * 8) * 2048 + y0 * 32 + lane;
    u32 v[8];
#pragma unroll
    for (int e = 0; e < 8; e++) v[e] = src[e * 2048];
    gu32* ep = (gu32*)(ws + OFF_CTL + gi * 128);
    u32 epoch = 0;
    if (wv == 0) epoch = __hip_atomic_load(ep, RLX_AGENT);
    u32 gw[4], bw[4];
#pragma unroll
    for (int e = 0; e < 4; e++) { gw[e] = g1[c8 * 4 + e]; bw[e] = be1[c8 * 4 + e]; }
    const float K = __builtin_bit_cast(float, __builtin_amdgcn_readfirstlane(v[0] << 16));
    float d[16];
    float s = 0.f, q = 0.f;
#pragma unroll
    for (int e = 0; e < 8; e++) {
      d[e] = bf_lo(v[e]) - K; d[8 + e] = bf_hi(v[e]) - K;
      s += d[e]; q = __builtin_fmaf(d[e], d[e], q);
      s += d[8 + e]; q = __builtin_fmaf(d[8 + e], d[8 + e], q);
    }
    s = wave_sum(s); q = wave_sum(q);
    if (lane == 0) { sm[2 * wv] = K + s * (1.f / 1024.f); sm[2 * wv + 1] = q - s * s * (1.f / 1024.f); }
    STAMP(1)
    __syncthreads();
    STAMP(2)
    if (wv == 0) {
      float mv = lane < 10 ? sm[2 * lane] : 0.f, m2 = lane < 10 ? sm[2 * lane + 1] : 0.f;
      float mean = row0_sum(mv) * 0.1f;
      float dd = lane < 10 ? mv - mean : 0.f;
      float M2 = row0_sum(m2 + 1024.f * dd * dd);
      const u32 tag = epoch + 0x9E3779B9u;
      float gm, rs;
      exchange_stats((gu64*)(ws + OFF_G1) + gi * 64, member, tag, mean, M2, lane, gm, rs);
      if (lane == 0) {
        sm[24] = gm; sm[25] = rs;
        if (member == 0) __hip_atomic_store(ep, tag, RLX_AGENT);
      }
    }
    STAMP(3)
    __syncthreads();
    STAMP(4)
    const float mu = sm[24] - K, rs = sm[25] * LOG2E;
    float o[16];
#pragma unroll
    for (int e = 0; e < 8; e++) {
      float ga = (e & 1) ? bf_hi(gw[e >> 1]) : bf_lo(gw[e >> 1]);
      float be = (e & 1) ? bf_hi(bw[e >> 1]) : bf_lo(bw[e >> 1]);
      float a = rs * ga, c = __builtin_fmaf(-mu, a, be * LOG2E);
      o[e] = silu_l2(__builtin_fmaf(d[e], a, c));
      o[8 + e] = silu_l2(__builtin_fmaf(d[8 + e], a, c));
    }
    u32x4 p0, p1;
#pragma unroll
    for (int e = 0; e < 4; e++) { p0[e] = pack_bf16(o[2 * e], o[2 * e + 1]); p1[e] = pack_bf16(o[8 + 2 * e], o[9 + 2 * e]); }
    u32x4* dst = (u32x4*)(ws + OFF_ACT1) + (size_t)(b * 40 + c8) * 4096 + y0 * 64 + lane * 2;
    dst[0] = p0; dst[1] = p1;
    STAMP(5)
  } else {
    const int a = bid * 2 + (wv - 10);
    if (a < 400) {
      const int cv = a >= 200, r = a - cv * 200;
      const int tile = r / 10, cg = r - tile * 10;
      const int og = tile / 5, n = tile - og * 5;
      const int i = lane & 15, c8 = cg * 4 + (lane >> 4);
      const int o = og * 80 + (cv ? n * 16 + i : chan1(n, i));
      const u32x4* src = (const u32x4*)((cv ? w2 : w1) + (size_t)o * 5760 + c8 * 144);
      u32 t[36];
#pragma unroll
      for (int k = 0; k < 9; k++) { u32x4 u = src[k]; t[4 * k] = u[0]; t[4 * k + 1] = u[1]; t[4 * k + 2] = u[2]; t[4 * k + 3] = u[3]; }
      __builtin_amdgcn_s_barrier();
      const int ks = c8 / 10, cl = c8 - ks * 10;
      const int q0 = 18 * (cl >> 1) + (cl & 1);
      u32x4* dbase = (u32x4*)(ws + (cv ? OFF_WP2 : OFF_WP1)) + ((og * 4 + ks) * 115 + n) * 64 + i;
#pragma unroll
      for (int tp = 0; tp < 9; tp++) {
        u32x4 f;
#pragma unroll
        for (int dd = 0; dd < 4; dd++) {
          const int e0 = 18 * dd + tp, e1 = e0 + 9;
          const u32 sel = ((e0 & 1) ? 0x0302u : 0x0100u) | ((e1 & 1) ? 0x07060000u : 0x05040000u);
          f[dd] = __builtin_amdgcn_perm(t[e1 >> 1], t[e0 >> 1], sel);
        }
        const int q = q0 + 2 * tp;
        dbase[(q >> 2) * 320 + (q & 3) * 16] = f;
      }
    } else if (a < 480) {
      const int r = a - 400, sl = r / 5, cg = r - sl * 5;
      const int e0 = sl * 72 + (lane >> 3);
      const u32x4* src = (const u32x4*)(wemb + (size_t)e0 * 640 + cg * 128 + (lane & 7) * 16);
      u32x4 u[9];
      u32 t0[9], t1[9];
#pragma unroll
      for (int k = 0; k < 9; k++) { u[k] = src[k * 320]; t0[k] = temb[e0 + 8 * k]; t1[k] = temb[1152 + e0 + 8 * k]; }
      __builtin_amdgcn_s_barrier();
      float a0[8], a1[8];
#pragma unroll
      for (int c = 0; c < 8; c++) { a0[c] = 0.f; a1[c] = 0.f; }
#pragma unroll
      for (int k = 0; k < 9; k++) {
        float s0 = silu_l2(bf_lo(t0[k]) * LOG2E), s1 = silu_l2(bf_lo(t1[k]) * LOG2E);
#pragma unroll
        for (int c = 0; c < 8; c++) {
          float wv_ = (c & 1) ? bf_hi(u[k][c >> 1]) : bf_lo(u[k][c >> 1]);
          a0[c] = __builtin_fmaf(s0, wv_, a0[c]); a1[c] = __builtin_fmaf(s1, wv_, a1[c]);
        }
      }
#pragma unroll
      for (int c = 0; c < 8; c++) {
        a0[c] = dpp_add<0x128, 0xf>(a0[c]); a1[c] = dpp_add<0x128, 0xf>(a1[c]);
        a0[c] += __shfl_xor(a0[c], 16); a1[c] += __shfl_xor(a1[c], 16);
        a0[c] += __shfl_xor(a0[c], 32); a1[c] += __shfl_xor(a1[c], 32);
      }
      if (lane < 8) {
        f32x4* d0 = (f32x4*)(ws + OFF_EMB) + (sl * 320 + cg * 64 + lane * 8) / 4;
        d0[0] = f32x4{a0[0], a0[1], a0[2], a0[3]}; d0[1] = f32x4{a0[4], a0[5], a0[6], a0[7]};
        d0[1280] = f32x4{a1[0], a1[1], a1[2], a1[3]}; d0[1281] = f32x4{a1[4], a1[5], a1[6], a1[7]};
      }
    } else {
      const int r = a - 480;
      u32x4* dbase = (u32x4*)(ws + ((r >> 4) ? OFF_WP2 : OFF_WP1)) + ((r & 15) * 115 + 110) * 64 + 32;
      const u32x4 z = {0u, 0u, 0u, 0u};
      if (lane < 32) {
#pragma unroll
        for (int n = 0; n < 5; n++) dbase[n * 64 + lane] = z;
      }
      if (r < 8) ((u64*)(ws + OFF_G2))[r * 64 + lane] = 0ull;
    }
    STAMP(6)
  }
  STAMP_OUT(0, wv)
}

template <int I> struct IC { static constexpr int value = I; };
template <int I, int N, class F>
__device__ __forceinline__ void static_for_(F&& f) {
  if constexpr (I < N) { f(IC<I>{}); static_for_<I + 1, N>(f); }
}
template <int N, class F>
__device__ __forceinline__ void static_for(F&& f) { static_for_<0, N>(f); }

constexpr int REG = 30720;
__host__ __device__ constexpr int a_off(int P) { return (P / 9) * 6144 + ((P % 9) / 3) * 288 + (P % 3) * 16; }
__host__ __device__ constexpr int a_dk(int s) { return s == 22 ? 0 : ((2 * s) % 9 == 8 ? 2 : ((2 * s) % 3 == 2 ? 1 : 0)); }

template <int CONV>
__global__ __launch_bounds__(256, 1) void k_conv(const char* __restrict__ act, const char* __restrict__ wp,
                                                 const char* __restrict__ p0, const char* __restrict__ p1,
                                                 char* __restrict__ ws, char* __restrict__ out) {
  __shared__ __attribute__((aligned(16))) char lds[4 * REG + 256];
  const int lane = threadIdx.x & 63;
  const int w = __builtin_amdgcn_readfirstlane(threadIdx.x >> 6);
  const int bid = blockIdx.x;
  const int xb = bid & 7, idx = bid >> 3;
  const int b = xb >> 2, band = xb & 3, og = idx & 3, tile = idx >> 2;
  const int y0 = band * 16 + (tile >> 2) * 8, x0 = (tile & 3) * 16;
  const int li = lane & 15, lj = lane >> 4;
  STAMP_DECL
  STAMP(0)

  const __amdgpu_buffer_rsrc_t rsA =
      __builtin_amdgcn_make_buffer_rsrc((void*)(act + (size_t)(b * 40 + w * 10) * 65536), 0, 10 * 65536, 0x00020000);
  const __amdgpu_buffer_rsrc_t rsB =
      __builtin_amdgcn_make_buffer_rsrc((void*)(wp + (size_t)(og * 4 + w) * 117760), 0, 117760, 0x00020000);
  u32 voffA[3];
#pragma unroll
  for (int i = 0; i < 3; i++) {
    const int p = i * 64 + lane, row = p / 18, px = p - row * 18;
    const int yy = y0 - 1 + row, xx = x0 - 1 + px;
    const bool ok = p < 180 && (u32)yy < 64u && (u32)xx < 64u;
    voffA[i] = ok ? (u32)(yy * 64 + xx) * 16u : 0x80000000u;
  }
  const u32 voffB = lane * 16;
  char* const ldsw = lds + w * REG + lane * 16;

  u32x4 st[2][6];
  u32x4 bf[4][5];
  u32x4 af[2][8];
#define LOAD_A(c, slot)                                                                         \
  _Pragma("unroll") for (int i_ = 0; i_ < 6; i_++) st[slot][i_] =                               \
      __builtin_amdgcn_raw_buffer_load_b128(rsA, voffA[i_ % 3], ((c) * 2 + i_ / 3) * 65536, 0);
#define WRITE_A(c, slot)                                                                        \
  _Pragma("unroll") for (int i_ = 0; i_ < 6; i_++) *(u32x4*)(ldsw + (c) * 6144 + i_ * 1024) = st[slot][i_];
#define LOAD_B(s, n) bf[(s) & 3][n] = __builtin_amdgcn_raw_buffer_load_b128(rsB, voffB, ((s) * 5 + (n)) * 1024, 0);

  LOAD_A(0, 0)
#pragma unroll
  for (int n = 0; n < 5; n++) { LOAD_B(0, n) }
#pragma unroll
  for (int n = 0; n < 5; n++) { LOAD_B(1, n) }
  LOAD_A(1, 1)
#pragma unroll
  for (int n = 0; n < 5; n++) { LOAD_B(2, n) }
#pragma unroll
  for (int n = 0; n < 5; n++) { LOAD_B(3, n) }

  f32x4 em[5];
  u32x4 gq[2], bq[2];
  u32x2 g4, b4;
  u32x2 xr[2][5];
  if (CONV == 1) {
    const float* ep = (const float*)(ws + OFF_EMB) + (b * 16 + li) * 320 + og * 80;
#pragma unroll
    for (int n = 0; n < 5; n++) em[n] = *(const f32x4*)(ep + chan1(n, 4 * lj));
#pragma unroll
    for (int t = 0; t < 2; t++) {
      gq[t] = *(const u32x4*)(p0 + (og * 80 + 32 * t + 8 * lj) * 2);
      bq[t] = *(const u32x4*)(p1 + (og * 80 + 32 * t + 8 * lj) * 2);
    }
    g4 = *(const u32x2*)(p0 + (og * 80 + 64 + 4 * lj) * 2);
    b4 = *(const u32x2*)(p1 + (og * 80 + 64 + 4 * lj) * 2);
  }

  const char* abase[4][3];
  {
    const char* lb = lds + w * REG + (lj & 1) * 3072 + li * 16;
    const bool hi = lj >> 1;
#pragma unroll
    for (int p = 0; p < 4; p++) {
      const char* r = lb + ((p + w) & 3) * 576;
      abase[p][0] = r + (hi ? 16 : 0); abase[p][1] = r + (hi ? 256 : 0); abase[p][2] = r + (hi ? 5536 : 0);
    }
  }
#define READ_A(s, m) af[(s) & 1][m] = *(const u32x4*)(abase[(m) >> 1][a_dk(s)] + a_off(2 * (s)) + ((m) & 1) * 288);

  f32x4 acc[8][5];
#pragma unroll
  for (int m = 0; m < 8; m++)
#pragma unroll
    for (int n = 0; n < 5; n++) acc[m][n] = f32x4{0.f, 0.f, 0.f, 0.f};

  WRITE_A(0, 0)
#pragma unroll
  for (int m = 0; m < 8; m++) { READ_A(0, m) }
  __builtin_amdgcn_sched_barrier(0);

  static_for<23>([&](auto sc) __attribute__((always_inline)) {
    constexpr int s = decltype(sc)::value;
    if (s == 1) STAMP(1)
    if (s == 4) STAMP(2)
    if (s == 8) STAMP(3)
    if (s == 12) STAMP(4)
    if (s == 16) STAMP(5)
    if (s == 20) STAMP(6)
#pragma unroll
    for (int n = 0; n < 5; n++) {
#pragma unroll
      for (int m = 0; m < 8; m++) {
        const bf16x8 xa = __builtin_bit_cast(bf16x8, af[s & 1][m]);
        const bf16x8 wb = __builtin_bit_cast(bf16x8, bf[s & 3][n]);
        if (CONV == 1) acc[m][n] = __builtin_amdgcn_mfma_f32_16x16x32_bf16(wb, xa, acc[m][n], 0, 0, 0);
        else acc[m][n] = __builtin_amdgcn_mfma_f32_16x16x32_bf16(xa, wb, acc[m][n], 0, 0, 0);
        if (s < 22 && n < 4 && (m == 1 || m == 5)) { READ_A(s + 1, 2 * n + (m >> 2)) __builtin_amdgcn_sched_barrier(0); }
        if (n == 4 && m < 6) {
          if (s == 1) { *(u32x4*)(ldsw + 1 * 6144 + m * 1024) = st[1][m]; }
          if (s == 6) { *(u32x4*)(ldsw + 2 * 6144 + m * 1024) = st[0][m]; }
          if (s == 10) { *(u32x4*)(ldsw + 3 * 6144 + m * 1024) = st[1][m]; }
          if (s == 15) { *(u32x4*)(ldsw + 4 * 6144 + m * 1024) = st[0][m]; }
          if (s == 0) st[0][m] = __builtin_amdgcn_raw_buffer_load_b128(rsA, voffA[m % 3], (4 + m / 3) * 65536, 0);
          if (s == 3) st[1][m] = __builtin_amdgcn_raw_buffer_load_b128(rsA, voffA[m % 3], (6 + m / 3) * 65536, 0);
          if (s == 7) st[0][m] = __builtin_amdgcn_raw_buffer_load_b128(rsA, voffA[m % 3], (8 + m / 3) * 65536, 0);
          if (CONV == 2 && (s == 18 || s == 19) && m < 5) {
            const int r = s & 1;
            xr[r][m] = *(const u32x2*)(p0 + ((size_t)(b * 320 + og * 80 + m * 16 + li) * 4096 + (y0 + 2 * w + r) * 64 + x0 + 4 * lj) * 2);
          }
          __builtin_amdgcn_sched_barrier(0);
        }
        if (CONV == 1 && s == 5 && m < 4) { em[n][m] = row_allsum(em[n][m]); }
      }
      if (s + 4 < 23) { LOAD_B(s + 4, n) }
      __builtin_amdgcn_sched_barrier(0);
    }
  });

  STAMP(7)
#pragma unroll
  for (int m = 2; m < 8; m++)
#pragma unroll
    for (int n = 0; n < 5; n++) *(f32x4*)(ldsw + ((m - 2) * 5 + n) * 1024) = acc[m][n];
  STAMP(8)
  __syncthreads();
  STAMP(9)
#pragma unroll
  for (int d = 1; d < 4; d++) {
    const char* src = lds + ((w - d) & 3) * REG + (d - 1) * 10240 + lane * 16;
#pragma unroll
    for (int r = 0; r < 2; r++)
#pragma unroll
      for (int n = 0; n < 5; n++) acc[r][n] += *(const f32x4*)(src + (r * 5 + n) * 1024);
  }

  STAMP(10)
  if (CONV == 1) {
    float* sm = (float*)(lds + 4 * REG);
    const float K = __builtin_bit_cast(float, __builtin_amdgcn_readfirstlane(__builtin_bit_cast(int, acc[0][0][0] + em[0][0])));
    float s = 0.f, q = 0.f;
#pragma unroll
    for (int n = 0; n < 5; n++)
#pragma unroll
      for (int k = 0; k < 4; k++) {
        const float ek = em[n][k] - K;
#pragma unroll
        for (int r = 0; r < 2; r++) {
          const float dv = acc[r][n][k] + ek;
          acc[r][n][k] = dv; s += dv; q = __builtin_fmaf(dv, dv, q);
        }
      }
    s = wave_sum(s); q = wave_sum(q);
    if (lane == 0) { sm[2 * w] = K + s * (1.f / 2560.f); sm[2 * w + 1] = q - s * s * (1.f / 2560.f); }
    __syncthreads();
    STAMP(11)
    if (w == 0) {
      float mv = lane < 4 ? sm[2 * lane] : 0.f, m2 = lane < 4 ? sm[2 * lane + 1] : 0.f;
      float mean = row0_sum(mv) * 0.25f;
      float dd = lane < 4 ? mv - mean : 0.f;
      float M2 = row0_sum(m2 + 2560.f * dd * dd);
      float gm, rs;
      exchange_stats((gu64*)(ws + OFF_G2) + (b * 4 + og) * 64, band * 8 + tile, 1u, mean, M2, lane, gm, rs);
      if (lane == 0) { sm[16] = gm; sm[17] = rs; }
    }
    float ga[5][4], be[5][4];
#pragma unroll
    for (int n = 0; n < 5; n++)
#pragma unroll
      for (int k = 0; k < 4; k++) {
        const u32 gw = n < 4 ? gq[n >> 1][(n & 1) * 2 + (k >> 1)] : g4[k >> 1];
        const u32 bw = n < 4 ? bq[n >> 1][(n & 1) * 2 + (k >> 1)] : b4[k >> 1];
        ga[n][k] = ((k & 1) ? bf_hi(gw) : bf_lo(gw)) * LOG2E;
        be[n][k] = ((k & 1) ? bf_hi(bw) : bf_lo(bw)) * LOG2E;
        acc[0][n][k] *= ga[n][k]; acc[1][n][k] *= ga[n][k];
      }
    __syncthreads();
    STAMP(12)
    const float rs = sm[17], mr = (sm[16] - K) * rs;
    u32 pk[2][5][2];
#pragma unroll
    for (int n = 0; n < 5; n++) {
      float o[2][4];
#pragma unroll
      for (int k = 0; k < 4; k++) {
        const float c = __builtin_fmaf(-mr, ga[n][k], be[n][k]);
        o[0][k] = silu_l2(__builtin_fmaf(acc[0][n][k], rs, c));
        o[1][k] = silu_l2(__builtin_fmaf(acc[1][n][k], rs, c));
      }
#pragma unroll
      for (int r = 0; r < 2; r++) { pk[r][n][0] = pack_bf16(o[r][0], o[r][1]); pk[r][n][1] = pack_bf16(o[r][2], o[r][3]); }
    }
#pragma unroll
    for (int r = 0; r < 2; r++) {
      char* dst = out + ((size_t)(b * 40 + og * 10) * 4096 + (y0 + 2 * w + r) * 64 + x0 + li) * 16;
#pragma unroll
      for (int t = 0; t < 2; t++)
        *(u32x4*)(dst + (size_t)(4 * t + lj) * 65536) = u32x4{pk[r][2 * t][0], pk[r][2 * t][1], pk[r][2 * t + 1][0], pk[r][2 * t + 1][1]};
      *(u32x2*)(dst + (size_t)(8 + (lj >> 1)) * 65536 + (lj & 1) * 8) = u32x2{pk[r][4][0], pk[r][4][1]};
    }
  } else {
#pragma unroll
    for (int r = 0; r < 2; r++)
#pragma unroll
      for (int n = 0; n < 5; n++) {
        u32x2 o;
        o[0] = pack_bf16(acc[r][n][0] + bf_lo(xr[r][n][0]), acc[r][n][1] + bf_hi(xr[r][n][0]));
        o[1] = pack_bf16(acc[r][n][2] + bf_lo(xr[r][n][1]), acc[r][n][3] + bf_hi(xr[r][n][1]));
        *(u32x2*)(out + ((size_t)(b * 320 + og * 80 + n * 16 + li) * 4096 + (y0 + 2 * w + r) * 64 + x0 + 4 * lj) * 2) = o;
      }
  }
  STAMP(13)
  STAMP_OUT(CONV, w)
}

extern "C" void kernel_launch(void* const* d_in, const int* in_sizes, int n_in, void* d_out, int out_size,
                              void* d_ws, size_t ws_size, hipStream_t stream) {
  char* ws = (char*)d_ws;
  k_prep<<<256, 768, 0, stream>>>((const u32*)d_in[0], (const u16*)d_in[1], (const u32*)d_in[2], (const u32*)d_in[3],
                                  (const char*)d_in[4], (const char*)d_in[5], (const char*)d_in[8], ws);
  k_conv<1><<<256, 256, 0, stream>>>(ws + OFF_ACT1, ws + OFF_WP1, (const char*)d_in[6], (const char*)d_in[7], ws,
                                     ws + OFF_ACT2);
  k_conv<2><<<256, 256, 0, stream>>>(ws + OFF_ACT2, ws + OFF_WP2, (const char*)d_in[0], (const char*)d_in[0], ws,
                                     (char*)d_out);
}
```

```cpp
#include <hip/hip_runtime.h>
#include <hip/hip_bf16.h>

typedef unsigned u32;
typedef unsigned long long u64;
typedef unsigned short u16;
typedef __attribute__((ext_vector_type(4))) unsigned u32x4;
typedef __attribute__((ext_vector_type(2))) unsigned u32x2;
typedef __attribute__((ext_vector_type(4))) float f32x4;
typedef __attribute__((ext_vector_type(2))) float f32x2;
typedef __attribute__((ext_vector_type(8))) __bf16 bf16x8;
typedef __attribute__((ext_vector_type(2))) __bf16 bf16x2;
#define GAS __attribute__((address_space(1)))
typedef GAS u32 gu32;
typedef GAS u64 gu64;
#define RLX_AGENT __ATOMIC_RELAXED, __HIP_MEMORY_SCOPE_AGENT

constexpr u32 ACT_BYTES = 5242880;
constexpr u32 WP_BYTES = 1884160;
constexpr u32 OFF_ACT1 = 0;
constexpr u32 OFF_ACT2 = ACT_BYTES;
constexpr u32 OFF_WP = 2 * ACT_BYTES;
constexpr u32 OFF_EMB = OFF_WP + 2 * WP_BYTES;
constexpr u32 OFF_CTL = OFF_EMB + 40960;
constexpr u32 C_G1 = 1024, C_G2 = C_G1 + 4096, C_AUX = C_G2 + 4096, C_AUX2 = C_AUX + 1024, CTL_BYTES = C_AUX2 + 1024;
#define STAMP_DECL
#define STAMP(i) {}
#define STAMP_OUT(wv)
constexpr float LOG2E = 1.4426950408889634f;
constexpr float EPS = 1e-5f;
constexpr u32 SPIN_CAP = 1u << 16;

__device__ __forceinline__ float bf_lo(u32 v) { return __builtin_bit_cast(float, v << 16); }
__device__ __forceinline__ float bf_hi(u32 v) { return __builtin_bit_cast(float, v & 0xffff0000u); }
__device__ __forceinline__ u32 pack_bf16(float lo, float hi) {
  f32x2 v = {lo, hi};
  return __builtin_bit_cast(u32, __builtin_convertvector(v, bf16x2));
}
__device__ __forceinline__ float silu_l2(float yl) {
  float e = __builtin_amdgcn_exp2f(-yl);
  return yl * __builtin_amdgcn_rcpf(__builtin_fmaf(e, LOG2E, LOG2E));
}
template <int CTRL, int RM>
__device__ __forceinline__ float dpp_add(float v) {
  int t = __builtin_amdgcn_update_dpp(0, __builtin_bit_cast(int, v), CTRL, RM, 0xf, true);
  return v + __builtin_bit_cast(float, t);
}
__device__ __forceinline__ float rdlane(float v, int l) {
  return __builtin_bit_cast(float, __builtin_amdgcn_readlane(__builtin_bit_cast(int, v), l));
}
__device__ __forceinline__ float wave_sum(float v) {
  v = dpp_add<0x111, 0xf>(v); v = dpp_add<0x112, 0xf>(v); v = dpp_add<0x114, 0xf>(v); v = dpp_add<0x118, 0xf>(v);
  v = dpp_add<0x142, 0xa>(v); v = dpp_add<0x143, 0xc>(v);
  return rdlane(v, 63);
}
__device__ __forceinline__ float row0_sum(float v) {
  v = dpp_add<0x111, 0xf>(v); v = dpp_add<0x112, 0xf>(v); v = dpp_add<0x114, 0xf>(v); v = dpp_add<0x118, 0xf>(v);
  return rdlane(v, 15);
}
__device__ __forceinline__ float row_allsum(float v) {
  v = dpp_add<0x128, 0xf>(v); v = dpp_add<0x124, 0xf>(v); v = dpp_add<0x122, 0xf>(v); v = dpp_add<0x121, 0xf>(v);
  return v;
}

typedef __amdgpu_buffer_rsrc_t rsrc_t;
__device__ __forceinline__ rsrc_t mk_rsrc(const void* p, u32 bytes) {
  return __builtin_amdgcn_make_buffer_rsrc((void*)p, 0, bytes, 0x00020000);
}
#define SC1 16
#define BLD(rs, vo, so, aux) __builtin_amdgcn_raw_buffer_load_b128(rs, vo, so, aux)
#define SB0 __builtin_amdgcn_sched_barrier(0);
#define BST(v, rs, vo, so) { __builtin_amdgcn_raw_buffer_store_b128(v, rs, vo, so, SC1); SB0 asm volatile("s_nop 1"); SB0 }
#define CFENCE asm volatile("" ::: "memory");
#define DRAIN asm volatile("s_waitcnt vmcnt(0)" ::: "memory");
#define WG_BARRIER { asm volatile("s_waitcnt lgkmcnt(0)" ::: "memory"); __builtin_amdgcn_s_barrier(); asm volatile("" ::: "memory"); }
constexpr u32 OOB = 0x80000000u;
constexpr u32 HOLE = 0xffffffffu;

__device__ __forceinline__ u32 pack_ok(float lo, float hi) { return min(pack_bf16(lo, hi), HOLE - 1u); }
__device__ __forceinline__ u32 umax3(u32 a, u32 b, u32 c) { return max(max(a, b), c); }
#define PIN(v) asm volatile("" : "+v"(v));
__device__ __forceinline__ void group_stats(u32x4 v, float& gmean, float& rstd) {
  const u32 um = v[0], uq = v[2];
  const float m0 = __builtin_bit_cast(float, __builtin_amdgcn_readfirstlane(um));
  float dd = __builtin_bit_cast(float, um) - m0;
  float qq = __builtin_fmaf(10240.f * dd, dd, __builtin_bit_cast(float, uq));
  dd = dpp_add<0x111, 0xf>(dd); qq = dpp_add<0x111, 0xf>(qq);
  dd = dpp_add<0x112, 0xf>(dd); qq = dpp_add<0x112, 0xf>(qq);
  dd = dpp_add<0x114, 0xf>(dd); qq = dpp_add<0x114, 0xf>(qq);
  dd = dpp_add<0x118, 0xf>(dd); qq = dpp_add<0x118, 0xf>(qq);
  const float ds = (rdlane(dd, 15) + rdlane(dd, 31)) * (1.f / 32.f);
  const float var = fmaxf(__builtin_fmaf((rdlane(qq, 15) + rdlane(qq, 31)), 1.f / 327680.f, -ds * ds), 0.f) + EPS;
  gmean = m0 + ds;
  const float r0 = __builtin_amdgcn_rsqf(var);
  rstd = __builtin_fmaf(0.5f * r0, __builtin_fmaf(-var * r0, r0, 1.f), r0);
}
__device__ __forceinline__ int chan1(int n, int i) {
  return n < 4 ? 32 * (n >> 1) + 8 * (i >> 2) + 4 * (n & 1) + (i & 3) : 64 + i;
}

template <int I> struct IC { static constexpr int value = I; };
template <int I, int N, class F>
__device__ __forceinline__ void static_for_(F&& f) {
  if constexpr (I < N) { f(IC<I>{}); static_for_<I + 1, N>(f); }
}
template <int N, class F>
__device__ __forceinline__ void static_for(F&& f) { static_for_<0, N>(f); }

constexpr int REG = 30720;
__host__ __device__ constexpr int a_off(int P) { return (P / 9) * 6144 + ((P % 9) / 3) * 288 + (P % 3) * 16; }
__host__ __device__ constexpr int a_dk(int s) { return s == 22 ? 0 : ((2 * s) % 9 == 8 ? 2 : ((2 * s) % 3 == 2 ? 1 : 0)); }

__global__ __launch_bounds__(256, 1) void k_all(const char* __restrict__ x, const char* __restrict__ temb,
                                                const u32* __restrict__ g1, const u32* __restrict__ be1,
                                                const char* __restrict__ w1, const char* __restrict__ wemb,
                                                const char* __restrict__ g2, const char* __restrict__ be2,
                                                const char* __restrict__ w2, char* ws, char* __restrict__ out) {
  __shared__ __attribute__((aligned(16))) char lds[4 * REG + 256];
  float* const sm = (float*)(lds + 4 * REG);
  const int lane = threadIdx.x & 63;
  const int w = __builtin_amdgcn_readfirstlane(threadIdx.x >> 6);
  const int bid = blockIdx.x;
  const int xb = bid & 7, idx = bid >> 3;
  const int b = xb >> 2, band = xb & 3, og = idx & 3, tile = idx >> 2;
  const int member = band * 8 + tile;
  const int y0 = band * 16 + (tile >> 2) * 8, x0 = (tile & 3) * 16;
  const int li = lane & 15, lj = lane >> 4;
  STAMP_DECL
  STAMP(0)
  u32 pf0, pf1;
  {
    u64 pc;
    u32 len;
    asm volatile("s_getpc_b64 %0\n.Lk_all_beg:\n s_mov_b32 %1, .Lk_all_end-.Lk_all_beg" : "=s"(pc), "=s"(len));
    const rsrc_t rsP = mk_rsrc((const void*)pc, len);
    pf0 = 0;
    pf1 = 0;
    SB0
  }
  asm volatile("" :: "s"(x), "s"(temb), "s"(g1), "s"(be1), "s"(w1), "s"(wemb), "s"(g2), "s"(be2), "s"(w2), "s"(ws), "s"(out));
  const u32x4 holes = {HOLE, HOLE, HOLE, HOLE};
  const rsrc_t rsC = mk_rsrc(ws + OFF_CTL, CTL_BYTES);
  u32 tag;

  auto exchange = [&](float s, float q, float K, u32 coff, auto mid) __attribute__((always_inline)) {
    s = dpp_add<0x111, 0xf>(s); q = dpp_add<0x111, 0xf>(q);
    s = dpp_add<0x112, 0xf>(s); q = dpp_add<0x112, 0xf>(q);
    s = dpp_add<0x114, 0xf>(s); q = dpp_add<0x114, 0xf>(q);
    s = dpp_add<0x118, 0xf>(s); q = dpp_add<0x118, 0xf>(q);
    if (li == 15) *(f32x2*)(sm + 2 * (w * 4 + lj)) = f32x2{K + s * (1.f / 640.f), q - s * s * (1.f / 640.f)};
    WG_BARRIER
    STAMP(coff == C_G1 ? 2 : 15)
    const u32 so = coff + (b * 4 + og) * 512;
    if (w == 0) {
      const f32x2 p = *(const f32x2*)(sm + 2 * li);
      const float m0 = rdlane(p[0], 0);
      float dd = p[0] - m0, qq = __builtin_fmaf(640.f * dd, dd, p[1]);
      dd = dpp_add<0x111, 0xf>(dd); qq = dpp_add<0x111, 0xf>(qq);
      dd = dpp_add<0x112, 0xf>(dd); qq = dpp_add<0x112, 0xf>(qq);
      dd = dpp_add<0x114, 0xf>(dd); qq = dpp_add<0x114, 0xf>(qq);
      dd = dpp_add<0x118, 0xf>(dd); qq = dpp_add<0x118, 0xf>(qq);
      const float mean = __builtin_fmaf(dd, 1.f / 16.f, m0), M2 = __builtin_fmaf(-40.f * dd, dd, qq);
      const u32x4 gr = {__builtin_bit_cast(u32, mean), tag, __builtin_bit_cast(u32, M2), tag};
      BST(gr, rsC, lane == 15 ? member * 16 : OOB, so);
    }
    SB0
    mid();
    SB0
    if (w == 0) {
      u32x4 pv;
      u32 spins = 0;
      do {
        pv = BLD(rsC, (lane & 31) * 16, so, SC1);
        CFENCE
      } while (__builtin_amdgcn_ballot_w64(pv[1] != tag || pv[3] != tag) != 0 && ++spins < SPIN_CAP);
      float gm, rs;
      group_stats(pv, gm, rs);
      *(f32x2*)(sm + 32) = f32x2{gm, rs};
    }
    STAMP(coff == C_G1 ? 4 : 26)
    WG_BARRIER
  };

  {
    const int yp = band * 16 + tile * 2 + (w & 1), c80 = og * 10 + (w >> 1);
    const rsrc_t rsX = mk_rsrc(x + (size_t)(b * 320 + c80 * 8) * 8192 + yp * 128, 72 * 8192);
    const u32 vx = (lane & 1) * 32768 + (lane >> 1) * 4;
    u32 v[5][4];
#pragma unroll
    for (int c = 0; c < 5; c++)
#pragma unroll
      for (int e = 0; e < 4; e++) v[c][e] = __builtin_amdgcn_raw_buffer_load_b32(rsX, vx, (c * 16 + e) * 8192, 0);
    const u32 epoch = __builtin_amdgcn_raw_buffer_load_b32(rsC, 0, 0, SC1);
    SB0
    u32x4 gw[5], bw[5];
    u32 vz = 0u;
    PIN(vz)
#pragma unroll
    for (int c = 0; c < 5; c++) { gw[c] = BLD(mk_rsrc(g1, 640), vz + c * 32, c80 * 16, 0); bw[c] = BLD(mk_rsrc(be1, 640), vz + c * 32, c80 * 16, 0); }
    CFENCE
    SB0
    STAMP(27)
    const rsrc_t rsO = mk_rsrc(ws + OFF_ACT1 + (u32)(b * 40 + c80) * 65536u + yp * 1024, 9 * 65536);
#pragma unroll
    for (int c = 0; c < 5; c++) BST(holes, rsO, lane * 16, c * 131072)
    SB0
    const int j = b * 32 + member;
    const int cv = w == 2, kind = w < 2 ? 2 : (j < 50 ? 0 : (j < 60 ? 1 : 2));
    const int u = 4 * j + lj, nt = u / 10, cl = u - nt * 10, tg = nt / 5, n = nt - tg * 5;
    const int r = kind == 1 ? (j - 50) + 10 * cv : 0, sl = og * 4 + r / 5, cg = r % 5;
    const rsrc_t rsE = mk_rsrc(temb, 4608);
    const u32 tw0 = __builtin_amdgcn_raw_buffer_load_b32(rsE, min(lane, 35) * 4, sl * 144, 0);
    const u32 tw1 = __builtin_amdgcn_raw_buffer_load_b32(rsE, min(lane, 35) * 4, sl * 144 + 2304, 0);
    const rsrc_t rsT = mk_rsrc(kind == 0 ? (cv ? w2 : w1) : wemb, kind == 0 ? 1843200 : 737280);
    const u32 vt = kind == 0 ? (u32)(tg * 80 + (cv ? n * 16 + li : chan1(n, li))) * 5760u + (og * 10 + cl) * 144
                             : (kind == 1 ? (u32)(sl * 72 + (lane >> 3)) * 640u + cg * 128 + (lane & 7) * 16 : OOB);
    const int tstep = kind == 0 ? 16 : 5120;
    u32x4 tv[9];
#pragma unroll
    for (int k = 0; k < 9; k++) tv[k] = BLD(rsT, vt, k * tstep, 0);
    SB0
    float ga[5][8], be[5][8];
    const u32 selA = (lane & 1) ? 0x03020c0cu : 0x05040c0cu, selB = (lane & 1) ? 0x07060c0cu : 0x01000c0cu;
    const float K = __builtin_bit_cast(float, __builtin_amdgcn_readfirstlane(v[0][0] << 16));
    float d[5][8];
    float s = 0.f, q = 0.f;
#pragma unroll
    for (int c = 0; c < 5; c++)
#pragma unroll
      for (int e = 0; e < 4; e++) {
        const u32 o = __builtin_amdgcn_update_dpp(0, (int)v[c][e], 0xb1, 0xf, 0xf, true);
        d[c][e] = __builtin_bit_cast(float, __builtin_amdgcn_perm(v[c][e], o, selA)) - K;
        d[c][4 + e] = __builtin_bit_cast(float, __builtin_amdgcn_perm(v[c][e], o, selB)) - K;
        s += d[c][e]; q = __builtin_fmaf(d[c][e], d[c][e], q);
        s += d[c][4 + e]; q = __builtin_fmaf(d[c][4 + e], d[c][4 + e], q);
      }
    tag = __builtin_amdgcn_readfirstlane(epoch) + 0x9E3779B9u;
    SB0
    asm volatile("s_waitcnt vmcnt(11)" :: "v"(pf0), "v"(pf1) : "memory");
    STAMP(1)
    auto repack = [&]() __attribute__((always_inline)) {
        const int q0 = 18 * (cl >> 1) + (cl & 1);
        const rsrc_t rsW = mk_rsrc(ws + OFF_WP + (u32)cv * WP_BYTES, WP_BYTES);
        const u32 dst = ((tg * 4 + og) * 115 + n) * 1024 + li * 16;
#pragma unroll
        for (int tp = 0; tp < 9; tp++) {
          u32x4 f;
#pragma unroll
          for (int dd = 0; dd < 4; dd++) {
            const int e0 = 18 * dd + tp, e1 = e0 + 9;
            const u32 sel = ((e0 & 1) ? 0x0302u : 0x0100u) | ((e1 & 1) ? 0x07060000u : 0x05040000u);
            f[dd] = __builtin_amdgcn_perm(tv[e1 >> 3][(e1 >> 1) & 3], tv[e0 >> 3][(e0 >> 1) & 3], sel);
          }
          const int qq = q0 + 2 * tp;
          BST(f, rsW, dst + (qq >> 2) * 5120 + (qq & 3) * 256, 0);
        }
    };
    exchange(s, q, K, C_G1, [&]() __attribute__((always_inline)) {
      if (kind == 0) {
        if (w == 3) repack();
      } else if (kind == 1) {
        float a0[8], a1[8];
#pragma unroll
        for (int c = 0; c < 8; c++) { a0[c] = 0.f; a1[c] = 0.f; }
#pragma unroll
        for (int k = 0; k < 9; k++) {
          const u32 t0 = __builtin_amdgcn_ds_bpermute((4 * k + (lane >> 4)) * 4, tw0);
          const u32 t1 = __builtin_amdgcn_ds_bpermute((4 * k + (lane >> 4)) * 4, tw1);
          const float s0 = silu_l2(((lane & 8) ? bf_hi(t0) : bf_lo(t0)) * LOG2E);
          const float s1 = silu_l2(((lane & 8) ? bf_hi(t1) : bf_lo(t1)) * LOG2E);
#pragma unroll
          for (int c = 0; c < 8; c++) {
            const float wv_ = (c & 1) ? bf_hi(tv[k][c >> 1]) : bf_lo(tv[k][c >> 1]);
            a0[c] = __builtin_fmaf(s0, wv_, a0[c]); a1[c] = __builtin_fmaf(s1, wv_, a1[c]);
          }
        }
        const bool hi = lane & 8, b1 = lane & 16;
        f32x4 o;
#pragma unroll
        for (int c = 0; c < 8; c++) {
          a0[c] = dpp_add<0x128, 0xf>(a0[c]); a1[c] = dpp_add<0x128, 0xf>(a1[c]);
          a0[c] += __shfl_xor(a0[c], 16); a1[c] += __shfl_xor(a1[c], 16);
        }
#pragma unroll
        for (int c = 0; c < 4; c++) {
          const float t_ = b1 ? (hi ? a1[4 + c] : a1[c]) : (hi ? a0[4 + c] : a0[c]);
          o[c] = t_ + __shfl_xor(t_, 32);
        }
        BST(__builtin_bit_cast(u32x4, o), mk_rsrc(ws + OFF_EMB, 40960),
            lane < 32 ? (u32)(((b1 ? 16 : 0) + sl) * 320 + cg * 64 + (lane & 7) * 8 + (hi ? 4 : 0)) * 4u : OOB, 0);
      }
#pragma unroll
    for (int c = 0; c < 5; c++)
#pragma unroll
      for (int e = 0; e < 4; e++) {
        const u32 gw_ = gw[c][e], bw_ = bw[c][e];
        ga[c][2 * e] = bf_lo(gw_) * LOG2E; ga[c][2 * e + 1] = bf_hi(gw_) * LOG2E;
        be[c][2 * e] = bf_lo(bw_) * LOG2E; be[c][2 * e + 1] = bf_hi(bw_) * LOG2E;
        PIN(ga[c][2 * e]) PIN(ga[c][2 * e + 1]) PIN(be[c][2 * e]) PIN(be[c][2 * e + 1])
      }
#pragma unroll
      for (int c = 0; c < 5; c++)
#pragma unroll
        for (int e = 0; e < 8; e++) { d[c][e] *= ga[c][e]; PIN(d[c][e]) }
      STAMP(3)
      if (w == 3) {
        DRAIN
        __builtin_amdgcn_raw_buffer_store_b32(tag, rsC, lane == 0 ? 0u : OOB, C_AUX + og * 256 + j * 4, SC1);
      }
    });
    STAMP(5)
    u32 p2 = __builtin_amdgcn_raw_buffer_load_b32(rsC, lane * 4, C_AUX + w * 256, SC1);
    SB0
    const f32x2 gs = *(const f32x2*)(sm + 32);
    const float rs = gs[1], mr = (gs[0] - K) * rs;
#pragma unroll
    for (int c = 0; c < 5; c++) {
      float o[8];
#pragma unroll
      for (int e = 0; e < 8; e++) o[e] = silu_l2(__builtin_fmaf(d[c][e], rs, __builtin_fmaf(-mr, ga[c][e], be[c][e])));
      const u32x4 p = {pack_ok(o[0], o[1]), pack_ok(o[2], o[3]), pack_ok(o[4], o[5]), pack_ok(o[6], o[7])};
      BST(p, rsO, lane * 16, c * 131072);
      SB0
    }
    STAMP(6)
    if (kind == 0 && w == 2) repack();
    for (u32 spins = 0; __builtin_amdgcn_ballot_w64(p2 != tag) != 0 && spins < SPIN_CAP; ++spins) {
      p2 = __builtin_amdgcn_raw_buffer_load_b32(rsC, lane * 4, C_AUX + w * 256, SC1);
      CFENCE
    }
    CFENCE
    STAMP(7)
  }

  u32 voffA[3];
#pragma unroll
  for (int i = 0; i < 3; i++) {
    const int p = i * 64 + lane, row = p / 18, px = p - row * 18;
    const int yy = y0 - 1 + row, xx = x0 - 1 + px;
    const bool ok = p < 180 && (u32)yy < 64u && (u32)xx < 64u;
    voffA[i] = ok ? (u32)(yy * 64 + xx) * 16u : OOB;
  }
  u32 voffB4 = lane * 16 + 4096;
  asm volatile("" : "+v"(voffB4));
  const u32 voffB = lane * 16, voffBl = lane < 32 ? lane * 16 : OOB;
  char* const ldsw = lds + w * REG + lane * 16;
  const char* abase[4][3];
  {
    const char* lb = lds + w * REG + (lj & 1) * 3072 + li * 16;
    const bool hi = lj >> 1;
#pragma unroll
    for (int p = 0; p < 4; p++) {
      const char* r = lb + ((p + w) & 3) * 576;
      abase[p][0] = r + (hi ? 16 : 0); abase[p][1] = r + (hi ? 256 : 0); abase[p][2] = r + (hi ? 5536 : 0);
    }
  }
  const rsrc_t rsB1 = mk_rsrc(ws + OFF_WP + (og * 4 + w) * 117760, 117760);
  const rsrc_t rsB2 = mk_rsrc(ws + OFF_WP + WP_BYTES + (og * 4 + w) * 117760, 117760);
  const rsrc_t rsO2 = mk_rsrc(ws + OFF_ACT2 + (u32)(b * 40 + og * 10) * 65536u + ((y0 + 2 * w) * 64 + x0) * 16, 10 * 65536);
  const u32 vo2 = lj * 65536 + li * 16, vo2l = (8 + (lj >> 1)) * 65536 + (lj & 1) * 1024 + li * 16;

  u32x4 st[2][6];
  u32x4 bf[4][5];
  u32x4 af[2][8];
  f32x4 acc[8][5];
  f32x4 em[5];
  u32x4 gq[2], bq[2];
  u32x2 g4, b4;
  u32x2 xr[2][5];
  u32x4 a2;
#define LOAD_A1(c, slot, i_) st[slot][i_] = BLD(rsA, voffA[(i_) % 3], ((c) * 2 + (i_) / 3) * 65536, SC1);
#define LOAD_A(c, slot) _Pragma("unroll") for (int i_ = 0; i_ < 6; i_++) { LOAD_A1(c, slot, i_) }
#define MAX4(mx, v) mx = umax3(umax3(mx, (v)[0], (v)[1]), (v)[2], (v)[3]);
#define CHECK_A(c)                                                                                \
  if (__builtin_expect(__builtin_amdgcn_ballot_w64(hm == HOLE) != 0, 0)) {                        \
    u32x4 t_[6];                                                                                  \
    u32 spins_ = 0;                                                                               \
    do {                                                                                          \
      _Pragma("unroll") for (int i_ = 0; i_ < 6; i_++) t_[i_] = BLD(rsA, voffA[i_ % 3], ((c) * 2 + i_ / 3) * 65536, SC1); \
      CFENCE                                                                                      \
      hm = 0u;                                                                                    \
      _Pragma("unroll") for (int i_ = 0; i_ < 6; i_++) { MAX4(hm, t_[i_]) }                       \
    } while (__builtin_amdgcn_ballot_w64(hm == HOLE) != 0 && ++spins_ < SPIN_CAP);                \
    _Pragma("unroll") for (int i_ = 0; i_ < 6; i_++) *(u32x4*)(ldsw + (c) * 6144 + i_ * 1024) = t_[i_]; \
  }                                                                                               \
  hm = 0u;
#define WRITE_A1(c, slot, i_) { *(u32x4*)(ldsw + (c) * 6144 + (i_) * 1024) = st[slot][i_]; MAX4(hm, st[slot][i_]) }
#define LOAD_B(rs, s, n) bf[(s) & 3][n] = (s) == 22 ? BLD(rs, voffBl, ((s) * 5 + (n)) * 1024, SC1) : BLD(rs, (n) < 4 ? voffB + (n) * 1024 : voffB4, (s) * 5120, SC1);
#define MFMA_(first, c, wt, px, WC)                                                                 \
  if (CONV == 1) {                                                                                  \
    if (first) asm volatile("v_mfma_f32_16x16x32_bf16 %0, %1, %2, 0" : "=&a"(c) : WC(wt), "v"(px));  \
    else asm volatile("v_mfma_f32_16x16x32_bf16 %0, %1, %2, %0" : "+a"(c) : WC(wt), "v"(px));        \
  } else {                                                                                          \
    if (first) asm volatile("v_mfma_f32_16x16x32_bf16 %0, %2, %1, 0" : "=&a"(c) : WC(wt), "v"(px));  \
    else asm volatile("v_mfma_f32_16x16x32_bf16 %0, %2, %1, %0" : "+a"(c) : WC(wt), "v"(px));        \
  }
#define MFMA(first, c, wt, px) if (s & 1) { MFMA_(first, c, wt, px, "a") } else { MFMA_(first, c, wt, px, "v") }
#define READ_A(s, m) af[(s) & 1][m] = *(const u32x4*)(abase[(m) >> 1][a_dk(s)] + a_off(2 * (s)) + ((m) & 1) * 288);

  auto conv = [&](auto cc) __attribute__((always_inline)) {
    constexpr int CONV = decltype(cc)::value;
    const rsrc_t rsA = mk_rsrc(ws + (CONV == 1 ? OFF_ACT1 : OFF_ACT2) + (u32)(b * 40 + w * 10) * 65536u, 10 * 65536);
    const rsrc_t rsB = CONV == 1 ? rsB1 : rsB2;
    if (CONV == 1) {
#pragma unroll
      for (int n = 0; n < 5; n++) { LOAD_B(rsB, 0, n) }
      SB0
    }
    LOAD_A(0, 0)
    SB0
    if (CONV == 1) {
#pragma unroll
      for (int n = 0; n < 5; n++) { LOAD_B(rsB, 1, n) }
      SB0
    }
    u32 hm = 0u;
#pragma unroll
    for (int i = 0; i < 6; i++) WRITE_A1(0, 0, i)
    SB0
    CHECK_A(0)
    STAMP(CONV == 1 ? 8 : 18)
    LOAD_A(1, 1)
    if (CONV == 1) {
#pragma unroll
      for (int n = 0; n < 5; n++) { LOAD_B(rsB, 2, n) }
#pragma unroll
      for (int n = 0; n < 5; n++) { LOAD_B(rsB, 3, n) }
    }
    SB0
#pragma unroll
    for (int m = 0; m < 8; m++) { READ_A(0, m) }
    SB0

    static_for<23>([&](auto sc) __attribute__((always_inline)) {
      constexpr int s = decltype(sc)::value;
      if (s == 1) STAMP(CONV == 1 ? 9 : 19)
      if (s == 4) STAMP(CONV == 1 ? 10 : 20)
      if (s == 12) STAMP(CONV == 1 ? 11 : 21)
      if (CONV == 1 && s == 9) {
        asm volatile("s_waitcnt vmcnt(20)" ::: "memory");
        __builtin_amdgcn_s_barrier();
        CFENCE
        __builtin_amdgcn_raw_buffer_store_b32(tag, rsC, threadIdx.x == 0 ? 0u : OOB, C_AUX2 + bid * 4, SC1);
      }
      if (CONV == 1 && s == 13) a2 = BLD(rsC, voffB, C_AUX2, SC1);
#pragma unroll
      for (int n = 0; n < 5; n++) {
#pragma unroll
        for (int m = 0; m < 8; m++) {
          MFMA(s == 0, acc[m][n], bf[s & 3][n], af[s & 1][m])
          if (s < 22 && n < 4 && (m == 1 || m == 5)) { READ_A(s + 1, 2 * n + (m >> 2)) SB0 }
          if (n == 4 && m < 6) {
            if (s == 2) WRITE_A1(1, 1, m)
            if (s == 6) WRITE_A1(2, 0, m)
            if (s == 10) WRITE_A1(3, 1, m)
            if (s == 15) WRITE_A1(4, 0, m)
            if (s == 0) { LOAD_A1(2, 0, m) }
            if (s == 3) { LOAD_A1(3, 1, m) }
            if (s == 7) { LOAD_A1(4, 0, m) }
            if (CONV == 1 && m < 5) {
              if (s == 4) BST(holes, rsO2, m < 4 ? vo2 : vo2l, m < 4 ? (m & 1) * 1024 + (m >> 1) * 262144 : 0)
              if (s == 16) em[m] = __builtin_bit_cast(f32x4, BLD(mk_rsrc(ws + OFF_EMB, 40960), ((b * 16 + li) * 320 + og * 80 + chan1(m, 4 * lj)) * 4, 0, SC1));
              if (s == 19) {
                if (m < 2) gq[m] = *(const u32x4*)(g2 + (og * 80 + 32 * m + 8 * lj) * 2);
                else if (m < 4) bq[m - 2] = *(const u32x4*)(be2 + (og * 80 + 32 * (m - 2) + 8 * lj) * 2);
                else { g4 = *(const u32x2*)(g2 + (og * 80 + 64 + 4 * lj) * 2); b4 = *(const u32x2*)(be2 + (og * 80 + 64 + 4 * lj) * 2); }
              }
            }
            if (CONV == 2 && (s == 18 || s == 19) && m < 5) {
              const int r = s & 1;
              xr[r][m] = *(const u32x2*)(x + ((size_t)(b * 320 + og * 80 + m * 16 + li) * 4096 + (y0 + 2 * w + r) * 64 + x0 + 4 * lj) * 2);
            }
            SB0
          }
          if (CONV == 1 && s == 20 && m < 4) { float e_ = row_allsum(em[n][m]); asm volatile("" : "+v"(e_)); em[n][m] = e_; }
        }
        if (s + 4 < 23) { LOAD_B(rsB, s + 4, n) }
        SB0
      }
      if (s == 2) { CHECK_A(1) }
      if (s == 6) { CHECK_A(2) }
      if (s == 10) { CHECK_A(3) }
      if (s == 15) { CHECK_A(4) }
      if (CONV == 1 && s == 15) {
        if (__builtin_expect(__builtin_amdgcn_ballot_w64(a2[0] != tag || a2[1] != tag || a2[2] != tag || a2[3] != tag) != 0, 0)) {
          u32 spins = 0;
          do {
            a2 = BLD(rsC, voffB, C_AUX2, SC1);
            CFENCE
          } while (__builtin_amdgcn_ballot_w64(a2[0] != tag || a2[1] != tag || a2[2] != tag || a2[3] != tag) != 0 && ++spins < SPIN_CAP);
        }
        CFENCE
      }
    });
    asm volatile("s_nop 15\n s_nop 7"
                 : "+a"(acc[0][0]), "+a"(acc[0][1]), "+a"(acc[0][2]), "+a"(acc[0][3]), "+a"(acc[0][4]),
                   "+a"(acc[1][0]), "+a"(acc[1][1]), "+a"(acc[1][2]), "+a"(acc[1][3]), "+a"(acc[1][4])
                 :: "memory");
    SB0
    STAMP(CONV == 1 ? 12 : 22)
    if (CONV == 1) {
#pragma unroll
      for (int s = 0; s < 4; s++)
#pragma unroll
        for (int n = 0; n < 5; n++) { LOAD_B(rsB2, s, n) }
      SB0
    }
#pragma unroll
    for (int m = 2; m < 8; m++)
#pragma unroll
      for (int n = 0; n < 5; n++) *(f32x4*)(ldsw + ((m - 2) * 5 + n) * 1024) = acc[m][n];
    WG_BARRIER
    STAMP(CONV == 1 ? 13 : 23)
    {
      f32x4 t[3][10];
#define RED_READ(d) _Pragma("unroll") for (int i = 0; i < 10; i++)                                 \
      t[(d) - 1][i] = *(const f32x4*)(lds + ((w - (d)) & 3) * REG + ((d) - 1) * 10240 + lane * 16 + i * 1024);
#define RED_ADD(d) _Pragma("unroll") for (int i = 0; i < 10; i++) acc[i / 5][i % 5] += t[(d) - 1][i];
      RED_READ(1) RED_READ(2) SB0
      RED_ADD(1) SB0
      RED_READ(3) SB0
      RED_ADD(2) RED_ADD(3)
    }
    STAMP(CONV == 1 ? 14 : 24)
  };

  conv(IC<1>{});
  {
    const float K = __builtin_bit_cast(float, __builtin_amdgcn_readfirstlane(__builtin_bit_cast(int, acc[0][0][0] + em[0][0])));
    float s = 0.f, q = 0.f;
#pragma unroll
    for (int n = 0; n < 5; n++)
#pragma unroll
      for (int k = 0; k < 4; k++) {
        const float ek = em[n][k] - K;
#pragma unroll
        for (int r = 0; r < 2; r++) {
          const float dv = acc[r][n][k] + ek;
          acc[r][n][k] = dv; s += dv; q = __builtin_fmaf(dv, dv, q);
        }
      }
    float ga[5][4], be[5][4];
    exchange(s, q, K, C_G2, [&]() __attribute__((always_inline)) {
#pragma unroll
      for (int n = 0; n < 5; n++)
#pragma unroll
        for (int k = 0; k < 4; k++) {
          const u32 gw = n < 4 ? gq[n >> 1][(n & 1) * 2 + (k >> 1)] : g4[k >> 1];
          const u32 bw = n < 4 ? bq[n >> 1][(n & 1) * 2 + (k >> 1)] : b4[k >> 1];
          ga[n][k] = ((k & 1) ? bf_hi(gw) : bf_lo(gw)) * LOG2E;
          be[n][k] = ((k & 1) ? bf_hi(bw) : bf_lo(bw)) * LOG2E;
          float t0 = acc[0][n][k] * ga[n][k], t1 = acc[1][n][k] * ga[n][k];
          PIN(ga[n][k]) PIN(be[n][k]) PIN(t0) PIN(t1)
          acc[0][n][k] = t0; acc[1][n][k] = t1;
        }
    });
    STAMP(16)
    const f32x2 gs = *(const f32x2*)(sm + 32);
    const float rs = gs[1], mr = (gs[0] - K) * rs;
    u32 pk[2][5][2];
#pragma unroll
    for (int n = 0; n < 5; n++) {
      float o[2][4];
#pragma unroll
      for (int k = 0; k < 4; k++) {
        const float c = __builtin_fmaf(-mr, ga[n][k], be[n][k]);
        o[0][k] = silu_l2(__builtin_fmaf(acc[0][n][k], rs, c));
        o[1][k] = silu_l2(__builtin_fmaf(acc[1][n][k], rs, c));
      }
#pragma unroll
      for (int r = 0; r < 2; r++) { pk[r][n][0] = pack_ok(o[r][0], o[r][1]); pk[r][n][1] = pack_ok(o[r][2], o[r][3]); }
      if (n & 1) {
#pragma unroll
        for (int r = 0; r < 2; r++) {
          const u32x4 p = {pk[r][n - 1][0], pk[r][n - 1][1], pk[r][n][0], pk[r][n][1]};
          BST(p, rsO2, vo2, r * 1024 + (n >> 1) * 262144);
        }
        SB0
      }
    }
    {
      const auto s0 = __builtin_amdgcn_permlane16_swap(pk[0][4][0], pk[1][4][0], false, false);
      const auto s1 = __builtin_amdgcn_permlane16_swap(pk[0][4][1], pk[1][4][1], false, false);
      const u32x4 p = {s0[0], s1[0], s0[1], s1[1]};
      BST(p, rsO2, vo2l, 0);
      SB0
    }
    CFENCE
    STAMP(17)
  }
  conv(IC<2>{});
#pragma unroll
  for (int r = 0; r < 2; r++)
#pragma unroll
    for (int n = 0; n < 5; n++) {
      u32x2 o;
      o[0] = pack_bf16(acc[r][n][0] + bf_lo(xr[r][n][0]), acc[r][n][1] + bf_hi(xr[r][n][0]));
      o[1] = pack_bf16(acc[r][n][2] + bf_lo(xr[r][n][1]), acc[r][n][3] + bf_hi(xr[r][n][1]));
      *(u32x2*)(out + ((size_t)(b * 320 + og * 80 + n * 16 + li) * 4096 + (y0 + 2 * w + r) * 64 + x0 + 4 * lj) * 2) = o;
    }
  __builtin_amdgcn_raw_buffer_store_b32(tag, rsC, (bid | threadIdx.x) == 0 ? 0u : OOB, 0, SC1);
  asm volatile(".Lk_all_end:");
  STAMP(25)
  STAMP_OUT(w)
}

extern "C" void kernel_launch(void* const* d_in, const int* in_sizes, int n_in, void* d_out, int out_size,
                              void* d_ws, size_t ws_size, hipStream_t stream) {
  k_all<<<256, 256, 0, stream>>>((const char*)d_in[0], (const char*)d_in[1], (const u32*)d_in[2], (const u32*)d_in[3],
                                 (const char*)d_in[4], (const char*)d_in[5], (const char*)d_in[6], (const char*)d_in[7],
                                 (const char*)d_in[8], (char*)d_ws, (char*)d_out);
}
```

```cpp
#include <hip/hip_runtime.h>
#include <hip/hip_bf16.h>

typedef unsigned u32;
typedef unsigned long long u64;
typedef unsigned short u16;
typedef __attribute__((ext_vector_type(4))) unsigned u32x4;
typedef __attribute__((ext_vector_type(2))) unsigned u32x2;
typedef __attribute__((ext_vector_type(4))) float f32x4;
typedef __attribute__((ext_vector_type(2))) float f32x2;
typedef __attribute__((ext_vector_type(8))) __bf16 bf16x8;
typedef __attribute__((ext_vector_type(2))) __bf16 bf16x2;
#define GAS __attribute__((address_space(1)))
typedef GAS u32 gu32;
typedef GAS u64 gu64;
#define RLX_AGENT __ATOMIC_RELAXED, __HIP_MEMORY_SCOPE_AGENT

constexpr u32 ACT_BYTES = 5242880;
constexpr u32 WP_BYTES = 1884160;
constexpr u32 OFF_ACT1 = 0;
constexpr u32 OFF_ACT2 = ACT_BYTES;
constexpr u32 OFF_WP = 2 * ACT_BYTES;
constexpr u32 OFF_EMB = OFF_WP + 2 * WP_BYTES;
constexpr u32 OFF_CTL = OFF_EMB + 40960;
constexpr u32 C_G1 = 1024, C_G2 = C_G1 + 16384, C_AUX = C_G2 + 16384, C_AUX2 = C_AUX + 16384, CTL_BYTES = C_AUX2 + 16384;
#define STAMP_DECL
#define STAMP(i) {}
#define STAMP_OUT(wv)
constexpr float LOG2E = 1.4426950408889634f;
constexpr float EPS = 1e-5f;
constexpr u32 SPIN_CAP = 1u << 16;

__device__ __forceinline__ float bf_lo(u32 v) { return __builtin_bit_cast(float, v << 16); }
__device__ __forceinline__ float bf_hi(u32 v) { return __builtin_bit_cast(float, v & 0xffff0000u); }
__device__ __forceinline__ u32 pack_bf16(float lo, float hi) {
  f32x2 v = {lo, hi};
  return __builtin_bit_cast(u32, __builtin_convertvector(v, bf16x2));
}
__device__ __forceinline__ float silu_l2(float yl) {
  float e = __builtin_amdgcn_exp2f(-yl);
  return yl * __builtin_amdgcn_rcpf(__builtin_fmaf(e, LOG2E, LOG2E));
}
template <int CTRL, int RM>
__device__ __forceinline__ float dpp_add(float v) {
  int t = __builtin_amdgcn_update_dpp(0, __builtin_bit_cast(int, v), CTRL, RM, 0xf, true);
  return v + __builtin_bit_cast(float, t);
}
__device__ __forceinline__ float rdlane(float v, int l) {
  return __builtin_bit_cast(float, __builtin_amdgcn_readlane(__builtin_bit_cast(int, v), l));
}
__device__ __forceinline__ float wave_sum(float v) {
  v = dpp_add<0x111, 0xf>(v); v = dpp_add<0x112, 0xf>(v); v = dpp_add<0x114, 0xf>(v); v = dpp_add<0x118, 0xf>(v);
  v = dpp_add<0x142, 0xa>(v); v = dpp_add<0x143, 0xc>(v);
  return rdlane(v, 63);
}
__device__ __forceinline__ float row0_sum(float v) {
  v = dpp_add<0x111, 0xf>(v); v = dpp_add<0x112, 0xf>(v); v = dpp_add<0x114, 0xf>(v); v = dpp_add<0x118, 0xf>(v);
  return rdlane(v, 15);
}
__device__ __forceinline__ float row_allsum(float v) {
  v = dpp_add<0x128, 0xf>(v); v = dpp_add<0x124, 0xf>(v); v = dpp_add<0x122, 0xf>(v); v = dpp_add<0x121, 0xf>(v);
  return v;
}

typedef __amdgpu_buffer_rsrc_t rsrc_t;
__device__ __forceinline__ rsrc_t mk_rsrc(const void* p, u32 bytes) {
  return __builtin_amdgcn_make_buffer_rsrc((void*)p, 0, bytes, 0x00020000);
}
#define SC1 16
#define BLD(rs, vo, so, aux) __builtin_amdgcn_raw_buffer_load_b128(rs, vo, so, aux)
#define SB0 __builtin_amdgcn_sched_barrier(0);
#define BST(v, rs, vo, so) { __builtin_amdgcn_raw_buffer_store_b128(v, rs, vo, so, SC1); SB0 asm volatile("s_nop 1"); SB0 }
#define CFENCE asm volatile("" ::: "memory");
#define DRAIN asm volatile("s_waitcnt vmcnt(0)" ::: "memory");
#define WG_BARRIER { asm volatile("s_waitcnt lgkmcnt(0)" ::: "memory"); __builtin_amdgcn_s_barrier(); asm volatile("" ::: "memory"); }
constexpr u32 OOB = 0x80000000u;
constexpr u32 HOLE = 0xffffffffu;

__device__ __forceinline__ u32 pack_ok(float lo, float hi) { return min(pack_bf16(lo, hi), HOLE - 1u); }
__device__ __forceinline__ u32 umax3(u32 a, u32 b, u32 c) { return max(max(a, b), c); }
#define PIN(v) asm volatile("" : "+v"(v));
__device__ __forceinline__ void group_stats(u32x4 v, float& gmean, float& rstd) {
  const u32 um = v[0], uq = v[2];
  const float m0 = __builtin_bit_cast(float, __builtin_amdgcn_readfirstlane(um));
  float dd = __builtin_bit_cast(float, um) - m0;
  float qq = __builtin_fmaf(10240.f * dd, dd, __builtin_bit_cast(float, uq));
  dd = dpp_add<0x111, 0xf>(dd); qq = dpp_add<0x111, 0xf>(qq);
  dd = dpp_add<0x112, 0xf>(dd); qq = dpp_add<0x112, 0xf>(qq);
  dd = dpp_add<0x114, 0xf>(dd); qq = dpp_add<0x114, 0xf>(qq);
  dd = dpp_add<0x118, 0xf>(dd); qq = dpp_add<0x118, 0xf>(qq);
  const float ds = (rdlane(dd, 15) + rdlane(dd, 31)) * (1.f / 32.f);
  const float var = fmaxf(__builtin_fmaf((rdlane(qq, 15) + rdlane(qq, 31)), 1.f / 327680.f, -ds * ds), 0.f) + EPS;
  gmean = m0 + ds;
  const float r0 = __builtin_amdgcn_rsqf(var);
  rstd = __builtin_fmaf(0.5f * r0, __builtin_fmaf(-var * r0, r0, 1.f), r0);
}
__device__ __forceinline__ int chan1(int n, int i) {
  return n < 4 ? 32 * (n >> 1) + 8 * (i >> 2) + 4 * (n & 1) + (i & 3) : 64 + i;
}

template <int I> struct IC { static constexpr int value = I; };
template <int I, int N, class F>
__device__ __forceinline__ void static_for_(F&& f) {
  if constexpr (I < N) { f(IC<I>{}); static_for_<I + 1, N>(f); }
}
template <int N, class F>
__device__ __forceinline__ void static_for(F&& f) { static_for_<0, N>(f); }

constexpr int REG = 30720;
__host__ __device__ constexpr int a_off(int P) { return (P / 9) * 6144 + ((P % 9) / 3) * 288 + (P % 3) * 16; }
__host__ __device__ constexpr int a_dk(int s) { return s == 22 ? 0 : ((2 * s) % 9 == 8 ? 2 : ((2 * s) % 3 == 2 ? 1 : 0)); }

__global__ __launch_bounds__(256, 1) void k_all(const char* __restrict__ x, const char* __restrict__ temb,
                                                const u32* __restrict__ g1, const u32* __restrict__ be1,
                                                const char* __restrict__ w1, const char* __restrict__ wemb,
                                                const char* __restrict__ g2, const char* __restrict__ be2,
                                                const char* __restrict__ w2, char* ws, char* __restrict__ out) {
  __shared__ __attribute__((aligned(16))) char lds[4 * REG + 256];
  float* const sm = (float*)(lds + 4 * REG);
  const int lane = threadIdx.x & 63;
  const int w = __builtin_amdgcn_readfirstlane(threadIdx.x >> 6);
  const int bid = blockIdx.x;
  const int xb = bid & 7, idx = bid >> 3;
  const int b = xb >> 2, band = xb & 3, og = idx & 3, tile = idx >> 2;
  const int member = band * 8 + tile;
  const int y0 = band * 16 + (tile >> 2) * 8, x0 = (tile & 3) * 16;
  const int li = lane & 15, lj = lane >> 4;
  STAMP_DECL
  STAMP(0)
  asm volatile("" :: "s"(x), "s"(temb), "s"(g1), "s"(be1), "s"(w1), "s"(wemb), "s"(g2), "s"(be2), "s"(w2), "s"(ws), "s"(out));
  const u32x4 holes = {HOLE, HOLE, HOLE, HOLE};
  const rsrc_t rsC = mk_rsrc(ws + OFF_CTL, CTL_BYTES);
  u32 tag;
  u32x4 tv[9];
  u32 wdst, wq0;
#define REPACK(tp, rs, dst)                                                                          \
  {                                                                                                  \
    u32x4 f_;                                                                                        \
    _Pragma("unroll") for (int dd = 0; dd < 4; dd++) {                                               \
      const int e0 = 18 * dd + (tp), e1 = e0 + 9;                                                    \
      const u32 sel = ((e0 & 1) ? 0x0302u : 0x0100u) | ((e1 & 1) ? 0x07060000u : 0x05040000u);      \
      f_[dd] = __builtin_amdgcn_perm(tv[e1 >> 3][(e1 >> 1) & 3], tv[e0 >> 3][(e0 >> 1) & 3], sel);   \
    }                                                                                                \
    const u32 qq_ = wq0 + 2 * (tp);                                                                  \
    BST(f_, rs, (dst) + (qq_ >> 2) * 5120 + (qq_ & 3) * 256, 0)                                      \
  }
  const u32 fo = ((bid >> 3) & 15) * 1024 + w * 256;

  auto exchange = [&](float s, float q, float K, u32 coff, auto mid) __attribute__((always_inline)) {
    s = dpp_add<0x111, 0xf>(s); q = dpp_add<0x111, 0xf>(q);
    s = dpp_add<0x112, 0xf>(s); q = dpp_add<0x112, 0xf>(q);
    s = dpp_add<0x114, 0xf>(s); q = dpp_add<0x114, 0xf>(q);
    s = dpp_add<0x118, 0xf>(s); q = dpp_add<0x118, 0xf>(q);
    if (li == 15) *(f32x2*)(sm + 2 * (w * 4 + lj)) = f32x2{K + s * (1.f / 640.f), q - s * s * (1.f / 640.f)};
    WG_BARRIER
    STAMP(coff == C_G1 ? 2 : 15)
    const u32 so = coff + (b * 4 + og) * 512;
    if (w == 0) {
      const f32x2 p = *(const f32x2*)(sm + 2 * li);
      const float m0 = rdlane(p[0], 0);
      float dd = p[0] - m0, qq = __builtin_fmaf(640.f * dd, dd, p[1]);
      dd = dpp_add<0x111, 0xf>(dd); qq = dpp_add<0x111, 0xf>(qq);
      dd = dpp_add<0x112, 0xf>(dd); qq = dpp_add<0x112, 0xf>(qq);
      dd = dpp_add<0x114, 0xf>(dd); qq = dpp_add<0x114, 0xf>(qq);
      dd = dpp_add<0x118, 0xf>(dd); qq = dpp_add<0x118, 0xf>(qq);
      const float mean = __builtin_fmaf(dd, 1.f / 16.f, m0), M2 = __builtin_fmaf(-40.f * dd, dd, qq);
      const u32x4 gr = {__builtin_bit_cast(u32, mean), tag, __builtin_bit_cast(u32, M2), tag};
      BST(gr, rsC, li == 15 ? member * 16 + lj * 4096 : OOB, so);
    }
    SB0
    mid();
    SB0
    if (w == 0) {
      u32x4 pv;
      u32 spins = 0;
      do {
        pv = BLD(rsC, (lane & 31) * 16, so + (member & 3) * 4096, SC1);
        CFENCE
      } while (__builtin_amdgcn_ballot_w64(pv[1] != tag || pv[3] != tag) != 0 && ++spins < SPIN_CAP);
      float gm, rs;
      group_stats(pv, gm, rs);
      *(f32x2*)(sm + 32) = f32x2{gm, rs};
    }
    STAMP(coff == C_G1 ? 4 : 26)
    WG_BARRIER
  };

  {
    const int yp = band * 16 + tile * 2 + (w & 1), c80 = og * 10 + (w >> 1);
    const rsrc_t rsX = mk_rsrc(x + (size_t)(b * 320 + c80 * 8) * 8192 + yp * 128, 72 * 8192);
    const u32 vx = (lane & 1) * 32768 + (lane >> 1) * 4;
    u32 v[5][4];
#pragma unroll
    for (int c = 0; c < 5; c++)
#pragma unroll
      for (int e = 0; e < 4; e++) v[c][e] = __builtin_amdgcn_raw_buffer_load_b32(rsX, vx, (c * 16 + e) * 8192, 0);
    const u32 epoch = __builtin_amdgcn_raw_buffer_load_b32(rsC, 0, 0, SC1);
    SB0
    u32x4 gw[5], bw[5];
    u32 vz = 0u;
    PIN(vz)
#pragma unroll
    for (int c = 0; c < 5; c++) { gw[c] = BLD(mk_rsrc(g1, 640), vz + c * 32, c80 * 16, 0); bw[c] = BLD(mk_rsrc(be1, 640), vz + c * 32, c80 * 16, 0); }
    CFENCE
    SB0
    STAMP(27)
    const rsrc_t rsO = mk_rsrc(ws + OFF_ACT1 + (u32)(b * 40 + c80) * 65536u + yp * 1024, 9 * 65536);
#pragma unroll
    for (int c = 0; c < 5; c++) BST(holes, rsO, lane * 16, c * 131072)
    SB0
    const int j = b * 32 + member;
    const int r2 = (j - 50) * 3 + w - 1;
    const int cv = w == 2, kind = j < 50 ? (w < 2 ? 2 : 0) : (w > 0 && r2 < 40 ? 1 : 2);
    const int u = 4 * j + lj, nt = u / 10, cl = u - nt * 10, tg = nt / 5, n = nt - tg * 5;
    const int r = kind == 1 ? r2 >> 1 : 0, bt = r2 & 1, sl = og * 4 + r / 5, cg = r % 5;
    const rsrc_t rsE = mk_rsrc(temb, 4608);
    const u32 tw0 = __builtin_amdgcn_raw_buffer_load_b32(rsE, min(lane, 35) * 4, sl * 144 + bt * 2304, 0);
    const rsrc_t rsT = mk_rsrc(kind == 0 ? (cv ? w2 : w1) : wemb, kind == 0 ? 1843200 : 737280);
    const u32 vt = kind == 0 ? (u32)(tg * 80 + (cv ? n * 16 + li : chan1(n, li))) * 5760u + (og * 10 + cl) * 144
                             : (kind == 1 ? (u32)(sl * 72 + (lane >> 3)) * 640u + cg * 128 + (lane & 7) * 16 : OOB);
    const int tstep = kind == 0 ? 16 : 5120;
#pragma unroll
    for (int k = 0; k < 9; k++) tv[k] = BLD(rsT, vt, k * tstep, 0);
    SB0
    float ga[5][8], be[5][8];
    const u32 selA = (lane & 1) ? 0x03020c0cu : 0x05040c0cu, selB = (lane & 1) ? 0x07060c0cu : 0x01000c0cu;
    const float K = __builtin_bit_cast(float, __builtin_amdgcn_readfirstlane(v[0][0] << 16));
    float d[5][8];
    float s = 0.f, q = 0.f;
#pragma unroll
    for (int c = 0; c < 5; c++)
#pragma unroll
      for (int e = 0; e < 4; e++) {
        const u32 o = __builtin_amdgcn_update_dpp(0, (int)v[c][e], 0xb1, 0xf, 0xf, true);
        d[c][e] = __builtin_bit_cast(float, __builtin_amdgcn_perm(v[c][e], o, selA)) - K;
        d[c][4 + e] = __builtin_bit_cast(float, __builtin_amdgcn_perm(v[c][e], o, selB)) - K;
        s += d[c][e]; q = __builtin_fmaf(d[c][e], d[c][e], q);
        s += d[c][4 + e]; q = __builtin_fmaf(d[c][4 + e], d[c][4 + e], q);
      }
    tag = __builtin_amdgcn_readfirstlane(epoch) + 0x9E3779B9u;
    SB0
    asm volatile("s_waitcnt vmcnt(10)" ::: "memory");
    STAMP(1)
    wq0 = 18 * (cl >> 1) + (cl & 1);
    wdst = ((tg * 4 + og) * 115 + n) * 1024 + li * 16;
    auto embed = [&]() __attribute__((always_inline)) {
      float a0[8];
#pragma unroll
      for (int c = 0; c < 8; c++) a0[c] = 0.f;
#pragma unroll
      for (int k = 0; k < 9; k++) {
        const u32 t0 = __builtin_amdgcn_ds_bpermute((4 * k + (lane >> 4)) * 4, tw0);
        const float s0 = silu_l2(((lane & 8) ? bf_hi(t0) : bf_lo(t0)) * LOG2E);
#pragma unroll
        for (int c = 0; c < 8; c++) a0[c] = __builtin_fmaf(s0, (c & 1) ? bf_hi(tv[k][c >> 1]) : bf_lo(tv[k][c >> 1]), a0[c]);
      }
      const bool hi = lane & 8;
      f32x4 o;
#pragma unroll
      for (int c = 0; c < 8; c++) a0[c] = dpp_add<0x128, 0xf>(a0[c]);
#pragma unroll
      for (int c = 0; c < 4; c++) {
        float l_ = a0[c], h_ = a0[4 + c];
        PIN(l_) PIN(h_)
        float t_ = hi ? h_ : l_;
        t_ += __shfl_xor(t_, 16);
        o[c] = t_ + __shfl_xor(t_, 32);
      }
      BST(__builtin_bit_cast(u32x4, o), mk_rsrc(ws + OFF_EMB, 40960),
          lane < 16 ? (u32)((bt * 16 + sl) * 320 + cg * 64 + (lane & 7) * 8 + (hi ? 4 : 0)) * 4u : OOB, 0)
    };
    exchange(s, q, K, C_G1, [&]() __attribute__((always_inline)) {
      if (kind == 0 && w == 3) {
#pragma unroll
        for (int tp = 0; tp < 9; tp++) REPACK(tp, mk_rsrc(ws + OFF_WP, WP_BYTES), wdst)
      }
      if (w == 3 && kind != 0)
        __builtin_amdgcn_raw_buffer_store_b32(tag, rsC, lane < 16 ? lane * 1024 : OOB, C_AUX + og * 256 + j * 4, SC1);
      if (kind == 1) embed();
#pragma unroll
    for (int c = 0; c < 5; c++)
#pragma unroll
      for (int e = 0; e < 4; e++) {
        const u32 gw_ = gw[c][e], bw_ = bw[c][e];
        ga[c][2 * e] = bf_lo(gw_) * LOG2E; ga[c][2 * e + 1] = bf_hi(gw_) * LOG2E;
        be[c][2 * e] = bf_lo(bw_) * LOG2E; be[c][2 * e + 1] = bf_hi(bw_) * LOG2E;
        PIN(ga[c][2 * e]) PIN(ga[c][2 * e + 1]) PIN(be[c][2 * e]) PIN(be[c][2 * e + 1])
      }
#pragma unroll
      for (int c = 0; c < 5; c++)
#pragma unroll
        for (int e = 0; e < 8; e++) { d[c][e] *= ga[c][e]; PIN(d[c][e]) }
      STAMP(3)
      if (w == 3 && kind == 0) {
        DRAIN
        __builtin_amdgcn_raw_buffer_store_b32(tag, rsC, lane < 16 ? lane * 1024 : OOB, C_AUX + og * 256 + j * 4, SC1);
      }
    });
    STAMP(5)
    u32 p2 = __builtin_amdgcn_raw_buffer_load_b32(rsC, lane * 4, C_AUX + fo, SC1), p3;
    SB0
    const f32x2 gs = *(const f32x2*)(sm + 32);
    const float rs = gs[1], mr = (gs[0] - K) * rs;
#pragma unroll
    for (int c = 0; c < 5; c++) {
      float o[8];
#pragma unroll
      for (int e = 0; e < 8; e++) o[e] = silu_l2(__builtin_fmaf(d[c][e], rs, __builtin_fmaf(-mr, ga[c][e], be[c][e])));
      const u32x4 p = {pack_ok(o[0], o[1]), pack_ok(o[2], o[3]), pack_ok(o[4], o[5]), pack_ok(o[6], o[7])};
      BST(p, rsO, lane * 16, c * 131072);
      if (c == 1) p3 = __builtin_amdgcn_raw_buffer_load_b32(rsC, lane * 4, C_AUX + fo, SC1);
      SB0
    }
    STAMP(6)
    if (kind != 0 || w != 2) wdst = OOB;
    if (__builtin_amdgcn_ballot_w64(p2 != tag) != 0) {
      for (u32 spins = 0; __builtin_amdgcn_ballot_w64(p3 != tag) != 0 && spins < SPIN_CAP; ++spins) {
        p3 = __builtin_amdgcn_raw_buffer_load_b32(rsC, lane * 4, C_AUX + fo, SC1);
        CFENCE
      }
    }
    CFENCE
    STAMP(7)
  }

  u32 voffA[3];
#pragma unroll
  for (int i = 0; i < 3; i++) {
    const int p = i * 64 + lane, row = p / 18, px = p - row * 18;
    const int yy = y0 - 1 + row, xx = x0 - 1 + px;
    const bool ok = p < 180 && (u32)yy < 64u && (u32)xx < 64u;
    voffA[i] = ok ? (u32)(yy * 64 + xx) * 16u : OOB;
  }
  u32 voffB4 = lane * 16 + 4096;
  asm volatile("" : "+v"(voffB4));
  const u32 voffB = lane * 16, voffBl = lane < 32 ? lane * 16 : OOB;
  char* const ldsw = lds + w * REG + lane * 16;
  const char* abase[4][3];
  {
    const char* lb = lds + w * REG + (lj & 1) * 3072 + li * 16;
    const bool hi = lj >> 1;
#pragma unroll
    for (int p = 0; p < 4; p++) {
      const char* r = lb + ((p + w) & 3) * 576;
      abase[p][0] = r + (hi ? 16 : 0); abase[p][1] = r + (hi ? 256 : 0); abase[p][2] = r + (hi ? 5536 : 0);
    }
  }
  const rsrc_t rsB1 = mk_rsrc(ws + OFF_WP + (og * 4 + w) * 117760, 117760);
  const rsrc_t rsB2 = mk_rsrc(ws + OFF_WP + WP_BYTES + (og * 4 + w) * 117760, 117760);
  const rsrc_t rsO2 = mk_rsrc(ws + OFF_ACT2 + (u32)(b * 40 + og * 10) * 65536u + ((y0 + 2 * w) * 64 + x0) * 16, 10 * 65536);
  const u32 vo2 = lj * 65536 + li * 16, vo2l = (8 + (lj >> 1)) * 65536 + (lj & 1) * 1024 + li * 16;

  u32x4 st[2][6];
  u32x4 bf[4][5];
  u32x4 af[2][8];
  f32x4 acc[8][5];
  f32x4 em[5];
  u32x4 gq[2], bq[2];
  u32x2 g4, b4;
  u32x2 xr[2][5];
  u32 a2;
#define LOAD_A1(c, slot, i_) st[slot][i_] = BLD(rsA, voffA[(i_) % 3], ((c) * 2 + (i_) / 3) * 65536, SC1);
#define LOAD_A(c, slot) _Pragma("unroll") for (int i_ = 0; i_ < 6; i_++) { LOAD_A1(c, slot, i_) }
#define MAX4(mx, v) mx = umax3(umax3(mx, (v)[0], (v)[1]), (v)[2], (v)[3]);
#define CHECK_A(c)                                                                                \
  if (__builtin_expect(__builtin_amdgcn_ballot_w64(hm == HOLE) != 0, 0)) {                        \
    u32x4 t_[6];                                                                                  \
    u32 spins_ = 0;                                                                               \
    do {                                                                                          \
      _Pragma("unroll") for (int i_ = 0; i_ < 6; i_++) t_[i_] = BLD(rsA, voffA[i_ % 3], ((c) * 2 + i_ / 3) * 65536, SC1); \
      CFENCE                                                                                      \
      hm = 0u;                                                                                    \
      _Pragma("unroll") for (int i_ = 0; i_ < 6; i_++) { MAX4(hm, t_[i_]) }                       \
    } while (__builtin_amdgcn_ballot_w64(hm == HOLE) != 0 && ++spins_ < SPIN_CAP);                \
    _Pragma("unroll") for (int i_ = 0; i_ < 6; i_++) *(u32x4*)(ldsw + (c) * 6144 + i_ * 1024) = t_[i_]; \
  }                                                                                               \
  hm = 0u;
#define WRITE_A1(c, slot, i_) { *(u32x4*)(ldsw + (c) * 6144 + (i_) * 1024) = st[slot][i_]; MAX4(hm, st[slot][i_]) }
#define LOAD_B(rs, s, n) bf[(s) & 3][n] = (s) == 22 ? BLD(rs, voffBl, ((s) * 5 + (n)) * 1024, SC1) : BLD(rs, (n) < 4 ? voffB + (n) * 1024 : voffB4, (s) * 5120, SC1);
#define MFMA_(first, c, wt, px, WC)                                                                 \
  if (CONV == 1) {                                                                                  \
    if (first) asm volatile("v_mfma_f32_16x16x32_bf16 %0, %1, %2, 0" : "=&a"(c) : WC(wt), "v"(px));  \
    else asm volatile("v_mfma_f32_16x16x32_bf16 %0, %1, %2, %0" : "+a"(c) : WC(wt), "v"(px));        \
  } else {                                                                                          \
    if (first) asm volatile("v_mfma_f32_16x16x32_bf16 %0, %2, %1, 0" : "=&a"(c) : WC(wt), "v"(px));  \
    else asm volatile("v_mfma_f32_16x16x32_bf16 %0, %2, %1, %0" : "+a"(c) : WC(wt), "v"(px));        \
  }
#define MFMA(first, c, wt, px) if (s & 1) { MFMA_(first, c, wt, px, "a") } else { MFMA_(first, c, wt, px, "v") }
#define READ_A(s, m) af[(s) & 1][m] = *(const u32x4*)(abase[(m) >> 1][a_dk(s)] + a_off(2 * (s)) + ((m) & 1) * 288);

  auto conv = [&](auto cc) __attribute__((always_inline)) {
    constexpr int CONV = decltype(cc)::value;
    const rsrc_t rsA = mk_rsrc(ws + (CONV == 1 ? OFF_ACT1 : OFF_ACT2) + (u32)(b * 40 + w * 10) * 65536u, 10 * 65536);
    const rsrc_t rsB = CONV == 1 ? rsB1 : rsB2;
    if (CONV == 1) {
#pragma unroll
      for (int n = 0; n < 5; n++) { LOAD_B(rsB, 0, n) }
      SB0
    }
    LOAD_A(0, 0)
    SB0
    if (CONV == 1) {
#pragma unroll
      for (int n = 0; n < 5; n++) { LOAD_B(rsB, 1, n) }
      SB0
    }
    u32 hm = 0u;
#pragma unroll
    for (int i = 0; i < 6; i++) WRITE_A1(0, 0, i)
    SB0
    CHECK_A(0)
    STAMP(CONV == 1 ? 8 : 18)
    LOAD_A(1, 1)
    if (CONV == 1) {
#pragma unroll
      for (int n = 0; n < 5; n++) { LOAD_B(rsB, 2, n) }
#pragma unroll
      for (int n = 0; n < 5; n++) { LOAD_B(rsB, 3, n) }
    }
    SB0
#pragma unroll
    for (int m = 0; m < 8; m++) { READ_A(0, m) }
    SB0

    static_for<23>([&](auto sc) __attribute__((always_inline)) {
      constexpr int s = decltype(sc)::value;
      if (s == 1) STAMP(CONV == 1 ? 9 : 19)
      if (s == 4) STAMP(CONV == 1 ? 10 : 20)
      if (s == 12) STAMP(CONV == 1 ? 11 : 21)
      if (CONV == 1 && s == 9) {
        asm volatile("s_waitcnt vmcnt(20)" ::: "memory");
        __builtin_amdgcn_s_barrier();
        CFENCE
        __builtin_amdgcn_raw_buffer_store_b32(tag, rsC, threadIdx.x < 16 ? threadIdx.x * 1024 : OOB, C_AUX2 + bid * 4, SC1);
      }
      if (CONV == 1 && s == 16) a2 = __builtin_amdgcn_raw_buffer_load_b32(rsC, lane * 4, C_AUX2 + fo, SC1);
      if constexpr (s == 22) {
#pragma unroll
        for (int t = 0; t < 40; t++) {
          const int m = (t / 5 + 2) & 7, n = t % 5;
          MFMA(false, acc[m][n], bf[s & 3][n], af[s & 1][m])
          if (t >= 3 && t < 33) *(f32x4*)(ldsw + (t - 3) * 1024) = acc[(t - 3) / 5 + 2][(t - 3) % 5];
          if (CONV == 1 && t < 30 && (t & 1) == 0) { const int i = t >> 1, s2 = i < 10 ? i / 5 : 3; LOAD_B(rsB2, s2, i % 5) }
          SB0
        }
        return;
      }
#pragma unroll
      for (int n = 0; n < 5; n++) {
#pragma unroll
        for (int m = 0; m < 8; m++) {
          MFMA(s == 0, acc[m][n], bf[s & 3][n], af[s & 1][m])
          if (s < 22 && n < 4 && (m == 1 || m == 5)) { READ_A(s + 1, 2 * n + (m >> 2)) SB0 }
          if (CONV == 1 && s >= 1 && s <= 3 && n < 3 && m == 3) REPACK((s - 1) * 3 + n, mk_rsrc(ws + OFF_WP + WP_BYTES, WP_BYTES), wdst)
          if (n == 4 && m < 6) {
            if (s == 2) WRITE_A1(1, 1, m)
            if (s == 6) WRITE_A1(2, 0, m)
            if (s == 10) WRITE_A1(3, 1, m)
            if (s == 15) WRITE_A1(4, 0, m)
            if (s == 0) { LOAD_A1(2, 0, m) }
            if (s == 3) { LOAD_A1(3, 1, m) }
            if (s == 7) { LOAD_A1(4, 0, m) }
            if (CONV == 1 && m < 5) {
              if (s == 4) BST(holes, rsO2, m < 4 ? vo2 : vo2l, m < 4 ? (m & 1) * 1024 + (m >> 1) * 262144 : 0)
              if (s == 19) em[m] = __builtin_bit_cast(f32x4, BLD(mk_rsrc(ws + OFF_EMB, 40960), ((b * 16 + li) * 320 + og * 80 + chan1(m, 4 * lj)) * 4, 0, SC1));
              if (s == 20) {
                if (m < 2) gq[m] = *(const u32x4*)(g2 + (og * 80 + 32 * m + 8 * lj) * 2);
                else if (m < 4) bq[m - 2] = *(const u32x4*)(be2 + (og * 80 + 32 * (m - 2) + 8 * lj) * 2);
                else { g4 = *(const u32x2*)(g2 + (og * 80 + 64 + 4 * lj) * 2); b4 = *(const u32x2*)(be2 + (og * 80 + 64 + 4 * lj) * 2); }
              }
            }
            if (CONV == 2 && (s == 18 || s == 19) && m < 5) {
              const int r = s & 1;
              xr[r][m] = *(const u32x2*)(x + ((size_t)(b * 320 + og * 80 + m * 16 + li) * 4096 + (y0 + 2 * w + r) * 64 + x0 + 4 * lj) * 2);
            }
            SB0
          }
          if (CONV == 1 && s == 21 && m >= 4) { float e_ = row_allsum(em[n][m - 4]); asm volatile("" : "+v"(e_)); em[n][m - 4] = e_; }
        }
        if (s + 4 < 23) { LOAD_B(rsB, s + 4, n) }
        SB0
      }
      if (s == 2) { CHECK_A(1) }
      if (s == 6) { CHECK_A(2) }
      if (s == 10) { CHECK_A(3) }
      if (s == 15) { CHECK_A(4) }
      if (CONV == 1 && s == 18) {
        if (__builtin_expect(__builtin_amdgcn_ballot_w64(a2 != tag) != 0, 0)) {
          u32 spins = 0;
          do {
            a2 = __builtin_amdgcn_raw_buffer_load_b32(rsC, lane * 4, C_AUX2 + fo, SC1);
            CFENCE
          } while (__builtin_amdgcn_ballot_w64(a2 != tag) != 0 && ++spins < SPIN_CAP);
        }
        CFENCE
        __builtin_amdgcn_s_barrier();
        CFENCE
      }
    });
    asm volatile("s_nop 15\n s_nop 7"
                 : "+a"(acc[0][0]), "+a"(acc[0][1]), "+a"(acc[0][2]), "+a"(acc[0][3]), "+a"(acc[0][4]),
                   "+a"(acc[1][0]), "+a"(acc[1][1]), "+a"(acc[1][2]), "+a"(acc[1][3]), "+a"(acc[1][4])
                 :: "memory");
    SB0
    STAMP(CONV == 1 ? 12 : 22)
    if (CONV == 1) {
#pragma unroll
      for (int n = 0; n < 5; n++) { LOAD_B(rsB2, 2, n) }
      SB0
    }
    WG_BARRIER
    STAMP(CONV == 1 ? 13 : 23)
    {
      f32x4 t[3][10];
#define RED_READ(d) _Pragma("unroll") for (int i = 0; i < 10; i++)                                 \
      t[(d) - 1][i] = *(const f32x4*)(lds + ((w - (d)) & 3) * REG + ((d) - 1) * 10240 + lane * 16 + i * 1024);
#define RED_ADD(d) _Pragma("unroll") for (int i = 0; i < 10; i++) acc[i / 5][i % 5] += t[(d) - 1][i];
      RED_READ(1) RED_READ(2) SB0
      RED_ADD(1) SB0
      RED_READ(3) SB0
      RED_ADD(2) RED_ADD(3)
    }
    STAMP(CONV == 1 ? 14 : 24)
  };

  conv(IC<1>{});
  {
    const float K = __builtin_bit_cast(float, __builtin_amdgcn_readfirstlane(__builtin_bit_cast(int, acc[0][0][0] + em[0][0])));
    float s = 0.f, q = 0.f;
#pragma unroll
    for (int n = 0; n < 5; n++)
#pragma unroll
      for (int k = 0; k < 4; k++) {
        const float ek = em[n][k] - K;
#pragma unroll
        for (int r = 0; r < 2; r++) {
          const float dv = acc[r][n][k] + ek;
          acc[r][n][k] = dv; s += dv; q = __builtin_fmaf(dv, dv, q);
        }
      }
    float ga[5][4], be[5][4];
    exchange(s, q, K, C_G2, [&]() __attribute__((always_inline)) {
#pragma unroll
      for (int n = 0; n < 5; n++)
#pragma unroll
        for (int k = 0; k < 4; k++) {
          const u32 gw = n < 4 ? gq[n >> 1][(n & 1) * 2 + (k >> 1)] : g4[k >> 1];
          const u32 bw = n < 4 ? bq[n >> 1][(n & 1) * 2 + (k >> 1)] : b4[k >> 1];
          ga[n][k] = ((k & 1) ? bf_hi(gw) : bf_lo(gw)) * LOG2E;
          be[n][k] = ((k & 1) ? bf_hi(bw) : bf_lo(bw)) * LOG2E;
          float t0 = acc[0][n][k] * ga[n][k], t1 = acc[1][n][k] * ga[n][k];
          PIN(ga[n][k]) PIN(be[n][k]) PIN(t0) PIN(t1)
          acc[0][n][k] = t0; acc[1][n][k] = t1;
        }
    });
    STAMP(16)
    const f32x2 gs = *(const f32x2*)(sm + 32);
    const float rs = gs[1], mr = (gs[0] - K) * rs;
    u32 pk[2][5][2];
#pragma unroll
    for (int n = 0; n < 5; n++) {
      float o[2][4];
#pragma unroll
      for (int k = 0; k < 4; k++) {
        const float c = __builtin_fmaf(-mr, ga[n][k], be[n][k]);
        o[0][k] = silu_l2(__builtin_fmaf(acc[0][n][k], rs, c));
        o[1][k] = silu_l2(__builtin_fmaf(acc[1][n][k], rs, c));
      }
#pragma unroll
      for (int r = 0; r < 2; r++) { pk[r][n][0] = pack_ok(o[r][0], o[r][1]); pk[r][n][1] = pack_ok(o[r][2], o[r][3]); }
      if (n & 1) {
#pragma unroll
        for (int r = 0; r < 2; r++) {
          const u32x4 p = {pk[r][n - 1][0], pk[r][n - 1][1], pk[r][n][0], pk[r][n][1]};
          BST(p, rsO2, vo2, r * 1024 + (n >> 1) * 262144);
        }
        SB0
      }
    }
    {
      const auto s0 = __builtin_amdgcn_permlane16_swap(pk[0][4][0], pk[1][4][0], false, false);
      const auto s1 = __builtin_amdgcn_permlane16_swap(pk[0][4][1], pk[1][4][1], false, false);
      const u32x4 p = {s0[0], s1[0], s0[1], s1[1]};
      BST(p, rsO2, vo2l, 0);
      SB0
    }
    CFENCE
    STAMP(17)
  }
  conv(IC<2>{});
  {
    char* const po = out + ((size_t)(b * 320 + og * 80 + li) * 4096 + (y0 + 2 * w + (lj & 1)) * 64 + x0 + 8 * (lj >> 1)) * 2;
#pragma unroll
    for (int n = 0; n < 5; n++) {
      u32 o[2][2];
#pragma unroll
      for (int r = 0; r < 2; r++) {
        o[r][0] = pack_bf16(acc[r][n][0] + bf_lo(xr[r][n][0]), acc[r][n][1] + bf_hi(xr[r][n][0]));
        o[r][1] = pack_bf16(acc[r][n][2] + bf_lo(xr[r][n][1]), acc[r][n][3] + bf_hi(xr[r][n][1]));
      }
      const auto s0 = __builtin_amdgcn_permlane16_swap(o[0][0], o[1][0], false, false);
      const auto s1 = __builtin_amdgcn_permlane16_swap(o[0][1], o[1][1], false, false);
      *(u32x4*)(po + (size_t)n * 131072) = u32x4{s0[0], s1[0], s0[1], s1[1]};
    }
  }
  __builtin_amdgcn_raw_buffer_store_b32(tag, rsC, (bid | threadIdx.x) == 0 ? 0u : OOB, 0, SC1);
  STAMP(25)
  STAMP_OUT(w)
}

extern "C" void kernel_launch(void* const* d_in, const int* in_sizes, int n_in, void* d_out, int out_size,
                              void* d_ws, size_t ws_size, hipStream_t stream) {
  k_all<<<256, 256, 0, stream>>>((const char*)d_in[0], (const char*)d_in[1], (const u32*)d_in[2], (const u32*)d_in[3],
                                 (const char*)d_in[4], (const char*)d_in[5], (const char*)d_in[6], (const char*)d_in[7],
                                 (const char*)d_in[8], (char*)d_ws, (char*)d_out);
}
```

```cpp
#include <hip/hip_runtime.h>
#include <hip/hip_bf16.h>

typedef unsigned u32;
typedef unsigned long long u64;
typedef unsigned short u16;
typedef __attribute__((ext_vector_type(4))) unsigned u32x4;
typedef __attribute__((ext_vector_type(2))) unsigned u32x2;
typedef __attribute__((ext_vector_type(4))) float f32x4;
typedef __attribute__((ext_vector_type(2))) float f32x2;
typedef __attribute__((ext_vector_type(8))) __bf16 bf16x8;
typedef __attribute__((ext_vector_type(2))) __bf16 bf16x2;
#define GAS __attribute__((address_space(1)))
typedef GAS u32 gu32;
typedef GAS u64 gu64;
#define RLX_AGENT __ATOMIC_RELAXED, __HIP_MEMORY_SCOPE_AGENT

constexpr u32 ACT_BYTES = 5242880;
constexpr u32 WP_BYTES = 1884160;
constexpr u32 OFF_ACT1 = 0;
constexpr u32 OFF_ACT2 = ACT_BYTES;
constexpr u32 OFF_WP = 2 * ACT_BYTES;
constexpr u32 OFF_EMB = OFF_WP + 2 * WP_BYTES;
constexpr u32 OFF_CTL = OFF_EMB + 40960;
constexpr u32 C_G1 = 1024, C_G2 = C_G1 + 16384, C_AUX = C_G2 + 16384, C_AUX2 = C_AUX + 16384, CTL_BYTES = C_AUX2 + 16384;
#define STAMP_DECL
#define STAMP(i) {}
#define STAMP_OUT(wv)
constexpr float LOG2E = 1.4426950408889634f;
constexpr float EPS = 1e-5f;
constexpr u32 SPIN_CAP = 1u << 16;

__device__ __forceinline__ float bf_lo(u32 v) { return __builtin_bit_cast(float, v << 16); }
__device__ __forceinline__ float bf_hi(u32 v) { return __builtin_bit_cast(float, v & 0xffff0000u); }
__device__ __forceinline__ u32 pack_bf16(float lo, float hi) {
  f32x2 v = {lo, hi};
  return __builtin_bit_cast(u32, __builtin_convertvector(v, bf16x2));
}
__device__ __forceinline__ float silu_l2(float yl) {
  float e = __builtin_amdgcn_exp2f(-yl);
  return yl * __builtin_amdgcn_rcpf(__builtin_fmaf(e, LOG2E, LOG2E));
}
template <int CTRL, int RM>
__device__ __forceinline__ float dpp_add(float v) {
  int t = __builtin_amdgcn_update_dpp(0, __builtin_bit_cast(int, v), CTRL, RM, 0xf, true);
  return v + __builtin_bit_cast(float, t);
}
__device__ __forceinline__ float rdlane(float v, int l) {
  return __builtin_bit_cast(float, __builtin_amdgcn_readlane(__builtin_bit_cast(int, v), l));
}
__device__ __forceinline__ float wave_sum(float v) {
  v = dpp_add<0x111, 0xf>(v); v = dpp_add<0x112, 0xf>(v); v = dpp_add<0x114, 0xf>(v); v = dpp_add<0x118, 0xf>(v);
  v = dpp_add<0x142, 0xa>(v); v = dpp_add<0x143, 0xc>(v);
  return rdlane(v, 63);
}
__device__ __forceinline__ float row0_sum(float v) {
  v = dpp_add<0x111, 0xf>(v); v = dpp_add<0x112, 0xf>(v); v = dpp_add<0x114, 0xf>(v); v = dpp_add<0x118, 0xf>(v);
  return rdlane(v, 15);
}
__device__ __forceinline__ float row_allsum(float v) {
  v = dpp_add<0x128, 0xf>(v); v = dpp_add<0x124, 0xf>(v); v = dpp_add<0x122, 0xf>(v); v = dpp_add<0x121, 0xf>(v);
  return v;
}

typedef __amdgpu_buffer_rsrc_t rsrc_t;
__device__ __forceinline__ rsrc_t mk_rsrc(const void* p, u32 bytes) {
  return __builtin_amdgcn_make_buffer_rsrc((void*)p, 0, bytes, 0x00020000);
}
#define SC1 16
#define BLD(rs, vo, so, aux) __builtin_amdgcn_raw_buffer_load_b128(rs, vo, so, aux)
#define SB0 __builtin_amdgcn_sched_barrier(0);
#define BST(v, rs, vo, so) { __builtin_amdgcn_raw_buffer_store_b128(v, rs, vo, so, SC1); SB0 asm volatile("s_nop 1"); SB0 }
#define CFENCE asm volatile("" ::: "memory");
#define DRAIN asm volatile("s_waitcnt vmcnt(0)" ::: "memory");
#define WG_BARRIER { asm volatile("s_waitcnt lgkmcnt(0)" ::: "memory"); __builtin_amdgcn_s_barrier(); asm volatile("" ::: "memory"); }
constexpr u32 OOB = 0x80000000u;
constexpr u32 HOLE = 0xffffffffu;

__device__ __forceinline__ u32 pack_ok(float lo, float hi) { return min(pack_bf16(lo, hi), HOLE - 1u); }
__device__ __forceinline__ u32 umax3(u32 a, u32 b, u32 c) { return max(max(a, b), c); }
#define PIN(v) asm volatile("" : "+v"(v));
__device__ __forceinline__ void group_stats(u32x4 v, float& gmean, float& rstd) {
  const u32 um = v[0], uq = v[2];
  const float m0 = __builtin_bit_cast(float, __builtin_amdgcn_readfirstlane(um));
  float dd = __builtin_bit_cast(float, um) - m0;
  float qq = __builtin_fmaf(10240.f * dd, dd, __builtin_bit_cast(float, uq));
  dd = dpp_add<0x111, 0xf>(dd); qq = dpp_add<0x111, 0xf>(qq);
  dd = dpp_add<0x112, 0xf>(dd); qq = dpp_add<0x112, 0xf>(qq);
  dd = dpp_add<0x114, 0xf>(dd); qq = dpp_add<0x114, 0xf>(qq);
  dd = dpp_add<0x118, 0xf>(dd); qq = dpp_add<0x118, 0xf>(qq);
  const float ds = (rdlane(dd, 15) + rdlane(dd, 31)) * (1.f / 32.f);
  const float var = fmaxf(__builtin_fmaf((rdlane(qq, 15) + rdlane(qq, 31)), 1.f / 327680.f, -ds * ds), 0.f) + EPS;
  gmean = m0 + ds;
  const float r0 = __builtin_amdgcn_rsqf(var);
  rstd = __builtin_fmaf(0.5f * r0, __builtin_fmaf(-var * r0, r0, 1.f), r0);
}
__device__ __forceinline__ int chan1(int n, int i) {
  return n < 4 ? 32 * (n >> 1) + 8 * (i >> 2) + 4 * (n & 1) + (i & 3) : 64 + i;
}

template <int I> struct IC { static constexpr int value = I; };
template <int I, int N, class F>
__device__ __forceinline__ void static_for_(F&& f) {
  if constexpr (I < N) { f(IC<I>{}); static_for_<I + 1, N>(f); }
}
template <int N, class F>
__device__ __forceinline__ void static_for(F&& f) { static_for_<0, N>(f); }

constexpr int REG = 30720;
__host__ __device__ constexpr int a_off(int P) { return (P / 9) * 6144 + ((P % 9) / 3) * 288 + (P % 3) * 16; }
__host__ __device__ constexpr int a_dk(int s) { return s == 22 ? 0 : ((2 * s) % 9 == 8 ? 2 : ((2 * s) % 3 == 2 ? 1 : 0)); }

__global__ __launch_bounds__(256, 1) void k_all(const char* __restrict__ x, const char* __restrict__ temb,
                                                const u32* __restrict__ g1, const u32* __restrict__ be1,
                                                const char* __restrict__ w1, const char* __restrict__ wemb,
                                                const char* __restrict__ g2, const char* __restrict__ be2,
                                                const char* __restrict__ w2, char* ws, char* __restrict__ out) {
  __shared__ __attribute__((aligned(16))) char lds[4 * REG + 256];
  float* const sm = (float*)(lds + 4 * REG);
  const int lane = threadIdx.x & 63;
  const int w = __builtin_amdgcn_readfirstlane(threadIdx.x >> 6);
  const int bid = blockIdx.x;
  const int xb = bid & 7, idx = bid >> 3;
  const int b = xb >> 2, band = xb & 3, og = idx & 3, tile = idx >> 2;
  const int member = band * 8 + tile;
  const int y0 = band * 16 + (tile >> 2) * 8, x0 = (tile & 3) * 16;
  const int li = lane & 15, lj = lane >> 4;
  STAMP_DECL
  STAMP(0)
  asm volatile("" :: "s"(x), "s"(temb), "s"(g1), "s"(be1), "s"(w1), "s"(wemb), "s"(g2), "s"(be2), "s"(w2), "s"(ws), "s"(out));
  const u32x4 holes = {HOLE, HOLE, HOLE, HOLE};
  const rsrc_t rsC = mk_rsrc(ws + OFF_CTL, CTL_BYTES);
  u32 tag;
  u32x4 tv[9];
  u32 wdst, wdst2, wq0;
  u32x4 wf;
#define REPACK(tp, rs, dst)                                                                          \
  {                                                                                                  \
    u32x4 f_;                                                                                        \
    _Pragma("unroll") for (int dd = 0; dd < 4; dd++) {                                               \
      const int e0 = 18 * dd + (tp), e1 = e0 + 9;                                                    \
      const u32 sel = ((e0 & 1) ? 0x0302u : 0x0100u) | ((e1 & 1) ? 0x07060000u : 0x05040000u);      \
      f_[dd] = __builtin_amdgcn_perm(tv[e1 >> 3][(e1 >> 1) & 3], tv[e0 >> 3][(e0 >> 1) & 3], sel);   \
    }                                                                                                \
    const u32 qq_ = wq0 + 2 * (tp);                                                                  \
    BST(f_, rs, (dst) + (qq_ >> 2) * 5120 + (qq_ & 3) * 256, 0)                                      \
  }
  const u32 fo = ((bid >> 3) & 15) * 1024 + w * 256;

  auto exchange = [&](float s, float q, float K, u32 coff, auto mid) __attribute__((always_inline)) {
    s = dpp_add<0x111, 0xf>(s); q = dpp_add<0x111, 0xf>(q);
    s = dpp_add<0x112, 0xf>(s); q = dpp_add<0x112, 0xf>(q);
    s = dpp_add<0x114, 0xf>(s); q = dpp_add<0x114, 0xf>(q);
    s = dpp_add<0x118, 0xf>(s); q = dpp_add<0x118, 0xf>(q);
    if (li == 15) *(f32x2*)(sm + 2 * (w * 4 + lj)) = f32x2{K + s * (1.f / 640.f), q - s * s * (1.f / 640.f)};
    WG_BARRIER
    STAMP(coff == C_G1 ? 2 : 15)
    const u32 so = coff + (b * 4 + og) * 512;
    if (w == 0) {
      const f32x2 p = *(const f32x2*)(sm + 2 * li);
      const float m0 = rdlane(p[0], 0);
      float dd = p[0] - m0, qq = __builtin_fmaf(640.f * dd, dd, p[1]);
      dd = dpp_add<0x111, 0xf>(dd); qq = dpp_add<0x111, 0xf>(qq);
      dd = dpp_add<0x112, 0xf>(dd); qq = dpp_add<0x112, 0xf>(qq);
      dd = dpp_add<0x114, 0xf>(dd); qq = dpp_add<0x114, 0xf>(qq);
      dd = dpp_add<0x118, 0xf>(dd); qq = dpp_add<0x118, 0xf>(qq);
      const float mean = __builtin_fmaf(dd, 1.f / 16.f, m0), M2 = __builtin_fmaf(-40.f * dd, dd, qq);
      const u32x4 gr = {__builtin_bit_cast(u32, mean), tag, __builtin_bit_cast(u32, M2), tag};
      BST(gr, rsC, li == 15 ? member * 16 + lj * 4096 : OOB, so);
    }
    SB0
    mid();
    SB0
    if (w == 0) {
      u32x4 pv;
      u32 spins = 0;
      do {
        pv = BLD(rsC, (lane & 31) * 16, so + (member & 3) * 4096, SC1);
        CFENCE
      } while (__builtin_amdgcn_ballot_w64(pv[1] != tag || pv[3] != tag) != 0 && ++spins < SPIN_CAP);
      float gm, rs;
      group_stats(pv, gm, rs);
      *(f32x2*)(sm + 32) = f32x2{gm, rs};
    }
    STAMP(coff == C_G1 ? 4 : 26)
    WG_BARRIER
  };

  {
    const int yp = band * 16 + tile * 2 + (w & 1), c80 = og * 10 + (w >> 1);
    const rsrc_t rsX = mk_rsrc(x + (size_t)(b * 320 + c80 * 8) * 8192 + yp * 128, 72 * 8192);
    const u32 vx = (lane & 1) * 32768 + (lane >> 1) * 4;
    u32 v[5][4];
#pragma unroll
    for (int c = 0; c < 5; c++)
#pragma unroll
      for (int e = 0; e < 4; e++) v[c][e] = __builtin_amdgcn_raw_buffer_load_b32(rsX, vx, (c * 16 + e) * 8192, 0);
    const u32 epoch = __builtin_amdgcn_raw_buffer_load_b32(rsC, 0, 0, SC1);
    SB0
    u32x4 gw[5], bw[5];
    u32 vz = 0u;
    PIN(vz)
#pragma unroll
    for (int c = 0; c < 5; c++) { gw[c] = BLD(mk_rsrc(g1, 640), vz + c * 32, c80 * 16, 0); bw[c] = BLD(mk_rsrc(be1, 640), vz + c * 32, c80 * 16, 0); }
    CFENCE
    SB0
    STAMP(27)
    const rsrc_t rsO = mk_rsrc(ws + OFF_ACT1 + (u32)(b * 40 + c80) * 65536u + yp * 1024, 9 * 65536);
#pragma unroll
    for (int c = 0; c < 5; c++) BST(holes, rsO, lane * 16, c * 131072)
    SB0
    const int j = b * 32 + member;
    const int r2 = (j - 50) * 3 + w - 1;
    const int cv = w == 2, kind = j < 50 ? (w < 2 ? 2 : 0) : (w > 0 && r2 < 40 ? 1 : 2);
    const int u = 4 * j + lj, nt = u / 10, cl = u - nt * 10, tg = nt / 5, n = nt - tg * 5;
    const int r = kind == 1 ? r2 >> 1 : 0, bt = r2 & 1, sl = og * 4 + r / 5, cg = r % 5;
    const rsrc_t rsE = mk_rsrc(temb, 4608);
    const u32 tw0 = __builtin_amdgcn_raw_buffer_load_b32(rsE, min(lane, 35) * 4, sl * 144 + bt * 2304, 0);
    const rsrc_t rsT = mk_rsrc(kind == 0 ? (cv ? w2 : w1) : wemb, kind == 0 ? 1843200 : 737280);
    const u32 vt = kind == 0 ? (u32)(tg * 80 + (cv ? n * 16 + li : chan1(n, li))) * 5760u + (og * 10 + cl) * 144
                             : (kind == 1 ? (u32)(sl * 72 + (lane >> 3)) * 640u + cg * 128 + (lane & 7) * 16 : OOB);
    const int tstep = kind == 0 ? 16 : 5120;
#pragma unroll
    for (int k = 0; k < 9; k++) tv[k] = BLD(rsT, vt, k * tstep, 0);
    SB0
    float ga[5][8], be[5][8];
    const u32 selA = (lane & 1) ? 0x03020c0cu : 0x05040c0cu, selB = (lane & 1) ? 0x07060c0cu : 0x01000c0cu;
    const float K = __builtin_bit_cast(float, __builtin_amdgcn_readfirstlane(v[0][0] << 16));
    float d[5][8];
    float s = 0.f, q = 0.f;
#pragma unroll
    for (int c = 0; c < 5; c++)
#pragma unroll
      for (int e = 0; e < 4; e++) {
        const u32 o = __builtin_amdgcn_update_dpp(0, (int)v[c][e], 0xb1, 0xf, 0xf, true);
        d[c][e] = __builtin_bit_cast(float, __builtin_amdgcn_perm(v[c][e], o, selA)) - K;
        d[c][4 + e] = __builtin_bit_cast(float, __builtin_amdgcn_perm(v[c][e], o, selB)) - K;
        s += d[c][e]; q = __builtin_fmaf(d[c][e], d[c][e], q);
        s += d[c][4 + e]; q = __builtin_fmaf(d[c][4 + e], d[c][4 + e], q);
      }
    tag = __builtin_amdgcn_readfirstlane(epoch) + 0x9E3779B9u;
    SB0
    asm volatile("s_waitcnt vmcnt(10)" ::: "memory");
    STAMP(1)
    wq0 = 18 * (cl >> 1) + (cl & 1);
    wdst = ((tg * 4 + og) * 115 + n) * 1024 + li * 16;
    auto embed = [&]() __attribute__((always_inline)) {
      float a0[8];
#pragma unroll
      for (int c = 0; c < 8; c++) a0[c] = 0.f;
#pragma unroll
      for (int k = 0; k < 9; k++) {
        const u32 t0 = __builtin_amdgcn_ds_bpermute((4 * k + (lane >> 4)) * 4, tw0);
        const float s0 = silu_l2(((lane & 8) ? bf_hi(t0) : bf_lo(t0)) * LOG2E);
#pragma unroll
        for (int c = 0; c < 8; c++) a0[c] = __builtin_fmaf(s0, (c & 1) ? bf_hi(tv[k][c >> 1]) : bf_lo(tv[k][c >> 1]), a0[c]);
      }
      const bool hi = lane & 8;
      f32x4 o;
#pragma unroll
      for (int c = 0; c < 8; c++) a0[c] = dpp_add<0x128, 0xf>(a0[c]);
#pragma unroll
      for (int c = 0; c < 4; c++) {
        float l_ = a0[c], h_ = a0[4 + c];
        PIN(l_) PIN(h_)
        float t_ = hi ? h_ : l_;
        t_ += __shfl_xor(t_, 16);
        o[c] = t_ + __shfl_xor(t_, 32);
      }
      BST(__builtin_bit_cast(u32x4, o), mk_rsrc(ws + OFF_EMB, 40960),
          lane < 16 ? (u32)((bt * 16 + sl) * 320 + cg * 64 + (lane & 7) * 8 + (hi ? 4 : 0)) * 4u : OOB, 0)
    };
    exchange(s, q, K, C_G1, [&]() __attribute__((always_inline)) {
      if (kind == 0 && w == 3) {
#pragma unroll
        for (int tp = 0; tp < 9; tp++) REPACK(tp, mk_rsrc(ws + OFF_WP, WP_BYTES), wdst)
      }
      if (w == 3 && kind != 0)
        __builtin_amdgcn_raw_buffer_store_b32(tag, rsC, lane < 16 ? lane * 1024 : OOB, C_AUX + og * 256 + j * 4, SC1);
      if (kind == 1) embed();
#pragma unroll
    for (int c = 0; c < 5; c++)
#pragma unroll
      for (int e = 0; e < 4; e++) {
        const u32 gw_ = gw[c][e], bw_ = bw[c][e];
        ga[c][2 * e] = bf_lo(gw_) * LOG2E; ga[c][2 * e + 1] = bf_hi(gw_) * LOG2E;
        be[c][2 * e] = bf_lo(bw_) * LOG2E; be[c][2 * e + 1] = bf_hi(bw_) * LOG2E;
        PIN(ga[c][2 * e]) PIN(ga[c][2 * e + 1]) PIN(be[c][2 * e]) PIN(be[c][2 * e + 1])
      }
#pragma unroll
      for (int c = 0; c < 5; c++)
#pragma unroll
        for (int e = 0; e < 8; e++) { d[c][e] *= ga[c][e]; PIN(d[c][e]) }
      STAMP(3)
      if (w == 3 && kind == 0) {
        DRAIN
        __builtin_amdgcn_raw_buffer_store_b32(tag, rsC, lane < 16 ? lane * 1024 : OOB, C_AUX + og * 256 + j * 4, SC1);
      }
    });
    STAMP(5)
    u32 p2 = __builtin_amdgcn_raw_buffer_load_b32(rsC, lane * 4, C_AUX + fo, SC1), p3;
    SB0
    const f32x2 gs = *(const f32x2*)(sm + 32);
    const float rs = gs[1], mr = (gs[0] - K) * rs;
#pragma unroll
    for (int c = 0; c < 5; c++) {
      float o[8];
#pragma unroll
      for (int e = 0; e < 8; e++) o[e] = silu_l2(__builtin_fmaf(d[c][e], rs, __builtin_fmaf(-mr, ga[c][e], be[c][e])));
      const u32x4 p = {pack_ok(o[0], o[1]), pack_ok(o[2], o[3]), pack_ok(o[4], o[5]), pack_ok(o[6], o[7])};
      BST(p, rsO, lane * 16, c * 131072);
      if (c == 1) p3 = __builtin_amdgcn_raw_buffer_load_b32(rsC, lane * 4, C_AUX + fo, SC1);
      SB0
    }
    STAMP(6)
    {
      const bool mine = kind == 0 && w == 2;
      const u32 q1 = wq0 + 2;
      wdst2 = mine ? wdst + (q1 >> 2) * 5120 + (q1 & 3) * 256 : OOB;
      wdst = mine ? wdst + (wq0 >> 2) * 5120 + (wq0 & 3) * 256 : OOB;
      PIN(wdst) PIN(wdst2)
    }
    if (__builtin_amdgcn_ballot_w64(p2 != tag) != 0) {
      for (u32 spins = 0; __builtin_amdgcn_ballot_w64(p3 != tag) != 0 && spins < SPIN_CAP; ++spins) {
        p3 = __builtin_amdgcn_raw_buffer_load_b32(rsC, lane * 4, C_AUX + fo, SC1);
        CFENCE
      }
    }
    CFENCE
    STAMP(7)
  }

  u32 voffA[3];
#pragma unroll
  for (int i = 0; i < 3; i++) {
    const int p = i * 64 + lane, row = p / 18, px = p - row * 18;
    const int yy = y0 - 1 + row, xx = x0 - 1 + px;
    const bool ok = p < 180 && (u32)yy < 64u && (u32)xx < 64u;
    voffA[i] = ok ? (u32)(yy * 64 + xx) * 16u : OOB;
  }
  u32 voffB4 = lane * 16 + 4096;
  asm volatile("" : "+v"(voffB4));
  const u32 voffB = lane * 16, voffBl = lane < 32 ? lane * 16 : OOB;
  char* const ldsw = lds + w * REG + lane * 16;
  const char* abase[4][3];
  {
    const char* lb = lds + w * REG + (lj & 1) * 3072 + li * 16;
    const bool hi = lj >> 1;
#pragma unroll
    for (int p = 0; p < 4; p++) {
      const char* r = lb + ((p + w) & 3) * 576;
      abase[p][0] = r + (hi ? 16 : 0); abase[p][1] = r + (hi ? 256 : 0); abase[p][2] = r + (hi ? 5536 : 0);
    }
  }
  const rsrc_t rsB1 = mk_rsrc(ws + OFF_WP + (og * 4 + w) * 117760, 117760);
  const rsrc_t rsB2 = mk_rsrc(ws + OFF_WP + WP_BYTES + (og * 4 + w) * 117760, 117760);
  const rsrc_t rsO2 = mk_rsrc(ws + OFF_ACT2 + (u32)(b * 40 + og * 10) * 65536u + ((y0 + 2 * w) * 64 + x0) * 16, 10 * 65536);
  const u32 vo2 = lj * 65536 + li * 16, vo2l = (8 + (lj >> 1)) * 65536 + (lj & 1) * 1024 + li * 16;

  u32x4 st[2][6];
  u32x4 bf[4][5];
  u32x4 af[2][8];
  f32x4 acc[8][5];
  f32x4 em[5];
  u32x4 gq[2], bq[2];
  u32x2 g4, b4;
  u32x2 xr[2][5];
  u32 a2;
#define LOAD_A1(c, slot, i_) st[slot][i_] = BLD(rsA, voffA[(i_) % 3], ((c) * 2 + (i_) / 3) * 65536, SC1);
#define LOAD_A(c, slot) _Pragma("unroll") for (int i_ = 0; i_ < 6; i_++) { LOAD_A1(c, slot, i_) }
#define MAX4(mx, v) mx = umax3(umax3(mx, (v)[0], (v)[1]), (v)[2], (v)[3]);
#define CHECK_A(c)                                                                                \
  if (__builtin_expect(__builtin_amdgcn_ballot_w64(hm == HOLE) != 0, 0)) {                        \
    u32x4 t_[6];                                                                                  \
    u32 spins_ = 0;                                                                               \
    do {                                                                                          \
      _Pragma("unroll") for (int i_ = 0; i_ < 6; i_++) t_[i_] = BLD(rsA, voffA[i_ % 3], ((c) * 2 + i_ / 3) * 65536, SC1); \
      CFENCE                                                                                      \
      hm = 0u;                                                                                    \
      _Pragma("unroll") for (int i_ = 0; i_ < 6; i_++) { MAX4(hm, t_[i_]) }                       \
    } while (__builtin_amdgcn_ballot_w64(hm == HOLE) != 0 && ++spins_ < SPIN_CAP);                \
    _Pragma("unroll") for (int i_ = 0; i_ < 6; i_++) *(u32x4*)(ldsw + (c) * 6144 + i_ * 1024) = t_[i_]; \
  }                                                                                               \
  hm = 0u;
#define WRITE_A1(c, slot, i_) { *(u32x4*)(ldsw + (c) * 6144 + (i_) * 1024) = st[slot][i_]; MAX4(hm, st[slot][i_]) }
#define LOAD_B(rs, s, n) bf[(s) & 3][n] = (s) == 22 ? BLD(rs, voffBl, ((s) * 5 + (n)) * 1024, SC1) : BLD(rs, (n) < 4 ? voffB + (n) * 1024 : voffB4, (s) * 5120, SC1);
#define MFMA_(first, c, wt, px, WC)                                                                 \
  if (CONV == 1) {                                                                                  \
    if (first) asm volatile("v_mfma_f32_16x16x32_bf16 %0, %1, %2, 0" : "=&a"(c) : WC(wt), "v"(px));  \
    else asm volatile("v_mfma_f32_16x16x32_bf16 %0, %1, %2, %0" : "+a"(c) : WC(wt), "v"(px));        \
  } else {                                                                                          \
    if (first) asm volatile("v_mfma_f32_16x16x32_bf16 %0, %2, %1, 0" : "=&a"(c) : WC(wt), "v"(px));  \
    else asm volatile("v_mfma_f32_16x16x32_bf16 %0, %2, %1, %0" : "+a"(c) : WC(wt), "v"(px));        \
  }
#define MFMA(first, c, wt, px) if (s & 1) { MFMA_(first, c, wt, px, "a") } else { MFMA_(first, c, wt, px, "v") }
#define READ_A(s, m) af[(s) & 1][m] = *(const u32x4*)(abase[(m) >> 1][a_dk(s)] + a_off(2 * (s)) + ((m) & 1) * 288);

  auto conv = [&](auto cc) __attribute__((always_inline)) {
    constexpr int CONV = decltype(cc)::value;
    const rsrc_t rsA = mk_rsrc(ws + (CONV == 1 ? OFF_ACT1 : OFF_ACT2) + (u32)(b * 40 + w * 10) * 65536u, 10 * 65536);
    const rsrc_t rsB = CONV == 1 ? rsB1 : rsB2;
    if (CONV == 1) {
#pragma unroll
      for (int n = 0; n < 5; n++) { LOAD_B(rsB, 0, n) }
      SB0
    }
    LOAD_A(0, 0)
    SB0
    if (CONV == 1) {
#pragma unroll
      for (int n = 0; n < 5; n++) { LOAD_B(rsB, 1, n) }
#pragma unroll
      for (int n = 0; n < 5; n++) { LOAD_B(rsB, 2, n) }
#pragma unroll
      for (int n = 0; n < 5; n++) { LOAD_B(rsB, 3, n) }
      SB0
    }
    u32 hm = 0u;
#pragma unroll
    for (int i = 0; i < 6; i++) WRITE_A1(0, 0, i)
    SB0
    CHECK_A(0)
    STAMP(CONV == 1 ? 8 : 18)
#pragma unroll
    for (int m = 0; m < 8; m++) { READ_A(0, m) }
    SB0
    LOAD_A(1, 1)
    SB0

    static_for<23>([&](auto sc) __attribute__((always_inline)) {
      constexpr int s = decltype(sc)::value;
      if (s == 1) STAMP(CONV == 1 ? 9 : 19)
      if (s == 4) STAMP(CONV == 1 ? 10 : 20)
      if (s == 12) STAMP(CONV == 1 ? 11 : 21)
      if (CONV == 1 && s == 9) {
        asm volatile("s_waitcnt vmcnt(20)" ::: "memory");
        __builtin_amdgcn_s_barrier();
        CFENCE
        __builtin_amdgcn_raw_buffer_store_b32(tag, rsC, threadIdx.x < 16 ? threadIdx.x * 1024 : OOB, C_AUX2 + bid * 4, SC1);
      }
      if (CONV == 1 && s == 16) a2 = __builtin_amdgcn_raw_buffer_load_b32(rsC, lane * 4, C_AUX2 + fo, SC1);
      if constexpr (s == 22) {
#pragma unroll
        for (int t = 0; t < 40; t++) {
          const int m = (t / 5 + 2) & 7, n = t % 5;
          MFMA(false, acc[m][n], bf[s & 3][n], af[s & 1][m])
          if (t >= 3 && t < 33) *(f32x4*)(ldsw + (t - 3) * 1024) = acc[(t - 3) / 5 + 2][(t - 3) % 5];
          if (CONV == 1 && t < 30 && (t & 1) == 0) { const int i = t >> 1, s2 = i < 10 ? i / 5 : 3; LOAD_B(rsB2, s2, i % 5) }
          SB0
        }
        return;
      }
#pragma unroll
      for (int n = 0; n < 5; n++) {
#pragma unroll
        for (int m = 0; m < 8; m++) {
          MFMA(s == 0, acc[m][n], bf[s & 3][n], af[s & 1][m])
          if (s < 22 && n < 4 && (m == 1 || m == 5)) { READ_A(s + 1, 2 * n + (m >> 2)) SB0 }
          if (CONV == 1 && s >= 1 && s <= 3 && n < 3) {
            const int tp = (s - 1) * 3 + n;
            if (m == 2 || m == 3) {
#pragma unroll
              for (int dd = 2 * m - 4; dd < 2 * m - 2; dd++) {
                const int e0 = 18 * dd + tp, e1 = e0 + 9;
                const u32 sel = ((e0 & 1) ? 0x0302u : 0x0100u) | ((e1 & 1) ? 0x07060000u : 0x05040000u);
                u32 f_ = __builtin_amdgcn_perm(tv[e1 >> 3][(e1 >> 1) & 3], tv[e0 >> 3][(e0 >> 1) & 3], sel);
                PIN(f_)
                wf[dd] = f_;
              }
              SB0
            }
            if (m == 6) BST(wf, mk_rsrc(ws + OFF_WP + WP_BYTES, WP_BYTES), (tp & 1) ? wdst2 : wdst, (tp >> 1) * 5120)
          }
          if (n == 4 && m < 6) {
            if (s == 2) WRITE_A1(1, 1, m)
            if (s == 6) WRITE_A1(2, 0, m)
            if (s == 10) WRITE_A1(3, 1, m)
            if (s == 15) WRITE_A1(4, 0, m)
            if (s == 0) { LOAD_A1(2, 0, m) }
            if (s == 3) { LOAD_A1(3, 1, m) }
            if (s == 7) { LOAD_A1(4, 0, m) }
            if (CONV == 1 && m < 5) {
              if (s == 4) BST(holes, rsO2, m < 4 ? vo2 : vo2l, m < 4 ? (m & 1) * 1024 + (m >> 1) * 262144 : 0)
              if (s == 19) em[m] = __builtin_bit_cast(f32x4, BLD(mk_rsrc(ws + OFF_EMB, 40960), ((b * 16 + li) * 320 + og * 80 + chan1(m, 4 * lj)) * 4, 0, SC1));
              if (s == 20) {
                if (m < 2) gq[m] = *(const u32x4*)(g2 + (og * 80 + 32 * m + 8 * lj) * 2);
                else if (m < 4) bq[m - 2] = *(const u32x4*)(be2 + (og * 80 + 32 * (m - 2) + 8 * lj) * 2);
                else { g4 = *(const u32x2*)(g2 + (og * 80 + 64 + 4 * lj) * 2); b4 = *(const u32x2*)(be2 + (og * 80 + 64 + 4 * lj) * 2); }
              }
            }
            if (CONV == 2 && (s == 18 || s == 19) && m < 5) {
              const int r = s & 1;
              xr[r][m] = *(const u32x2*)(x + ((size_t)(b * 320 + og * 80 + m * 16 + li) * 4096 + (y0 + 2 * w + r) * 64 + x0 + 4 * lj) * 2);
            }
            SB0
          }
          if (CONV == 1 && s == 21 && m >= 4) { float e_ = row_allsum(em[n][m - 4]); asm volatile("" : "+v"(e_)); em[n][m - 4] = e_; }
        }
        if (s + 4 < 23) { LOAD_B(rsB, s + 4, n) }
        SB0
      }
      if (s == 2) { CHECK_A(1) }
      if (s == 6) { CHECK_A(2) }
      if (s == 10) { CHECK_A(3) }
      if (s == 15) { CHECK_A(4) }
      if (CONV == 1 && s == 18) {
        if (__builtin_expect(__builtin_amdgcn_ballot_w64(a2 != tag) != 0, 0)) {
          u32 spins = 0;
          do {
            a2 = __builtin_amdgcn_raw_buffer_load_b32(rsC, lane * 4, C_AUX2 + fo, SC1);
            CFENCE
          } while (__builtin_amdgcn_ballot_w64(a2 != tag) != 0 && ++spins < SPIN_CAP);
        }
        CFENCE
        __builtin_amdgcn_s_barrier();
        CFENCE
      }
    });
    asm volatile("s_nop 15\n s_nop 7"
                 : "+a"(acc[0][0]), "+a"(acc[0][1]), "+a"(acc[0][2]), "+a"(acc[0][3]), "+a"(acc[0][4]),
                   "+a"(acc[1][0]), "+a"(acc[1][1]), "+a"(acc[1][2]), "+a"(acc[1][3]), "+a"(acc[1][4])
                 :: "memory");
    SB0
    STAMP(CONV == 1 ? 12 : 22)
    if (CONV == 1) {
#pragma unroll
      for (int n = 0; n < 5; n++) { LOAD_B(rsB2, 2, n) }
      SB0
    }
    WG_BARRIER
    STAMP(CONV == 1 ? 13 : 23)
    {
      f32x4 t[3][10];
#define RED_READ(d) _Pragma("unroll") for (int i = 0; i < 10; i++)                                 \
      t[(d) - 1][i] = *(const f32x4*)(lds + ((w - (d)) & 3) * REG + ((d) - 1) * 10240 + lane * 16 + i * 1024);
#define RED_ADD(d) _Pragma("unroll") for (int i = 0; i < 10; i++) acc[i / 5][i % 5] += t[(d) - 1][i];
      RED_READ(1) RED_READ(2) SB0
      RED_ADD(1) SB0
      RED_READ(3) SB0
      RED_ADD(2) RED_ADD(3)
    }
    STAMP(CONV == 1 ? 14 : 24)
  };

  conv(IC<1>{});
  {
    const float K = __builtin_bit_cast(float, __builtin_amdgcn_readfirstlane(__builtin_bit_cast(int, acc[0][0][0] + em[0][0])));
    float s = 0.f, q = 0.f;
#pragma unroll
    for (int n = 0; n < 5; n++)
#pragma unroll
      for (int k = 0; k < 4; k++) {
        const float ek = em[n][k] - K;
#pragma unroll
        for (int r = 0; r < 2; r++) {
          const float dv = acc[r][n][k] + ek;
          acc[r][n][k] = dv; s += dv; q = __builtin_fmaf(dv, dv, q);
        }
      }
    float ga[5][4], be[5][4];
    exchange(s, q, K, C_G2, [&]() __attribute__((always_inline)) {
#pragma unroll
      for (int n = 0; n < 5; n++)
#pragma unroll
        for (int k = 0; k < 4; k++) {
          const u32 gw = n < 4 ? gq[n >> 1][(n & 1) * 2 + (k >> 1)] : g4[k >> 1];
          const u32 bw = n < 4 ? bq[n >> 1][(n & 1) * 2 + (k >> 1)] : b4[k >> 1];
          ga[n][k] = ((k & 1) ? bf_hi(gw) : bf_lo(gw)) * LOG2E;
          be[n][k] = ((k & 1) ? bf_hi(bw) : bf_lo(bw)) * LOG2E;
          float t0 = acc[0][n][k] * ga[n][k], t1 = acc[1][n][k] * ga[n][k];
          PIN(ga[n][k]) PIN(be[n][k]) PIN(t0) PIN(t1)
          acc[0][n][k] = t0; acc[1][n][k] = t1;
        }
    });
    STAMP(16)
    const f32x2 gs = *(const f32x2*)(sm + 32);
    const float rs = gs[1], mr = (gs[0] - K) * rs;
    u32 pk[2][5][2];
#pragma unroll
    for (int n = 0; n < 5; n++) {
      float o[2][4];
#pragma unroll
      for (int k = 0; k < 4; k++) {
        const float c = __builtin_fmaf(-mr, ga[n][k], be[n][k]);
        o[0][k] = silu_l2(__builtin_fmaf(acc[0][n][k], rs, c));
        o[1][k] = silu_l2(__builtin_fmaf(acc[1][n][k], rs, c));
      }
#pragma unroll
      for (int r = 0; r < 2; r++) { pk[r][n][0] = pack_ok(o[r][0], o[r][1]); pk[r][n][1] = pack_ok(o[r][2], o[r][3]); }
      if (n & 1) {
#pragma unroll
        for (int r = 0; r < 2; r++) {
          const u32x4 p = {pk[r][n - 1][0], pk[r][n - 1][1], pk[r][n][0], pk[r][n][1]};
          BST(p, rsO2, vo2, r * 1024 + (n >> 1) * 262144);
        }
        SB0
      }
    }
    {
      const auto s0 = __builtin_amdgcn_permlane16_swap(pk[0][4][0], pk[1][4][0], false, false);
      const auto s1 = __builtin_amdgcn_permlane16_swap(pk[0][4][1], pk[1][4][1], false, false);
      const u32x4 p = {s0[0], s1[0], s0[1], s1[1]};
      BST(p, rsO2, vo2l, 0);
      SB0
    }
    CFENCE
    STAMP(17)
  }
  conv(IC<2>{});
  {
    char* const po = out + ((size_t)(b * 320 + og * 80 + li) * 4096 + (y0 + 2 * w + (lj & 1)) * 64 + x0 + 8 * (lj >> 1)) * 2;
#pragma unroll
    for (int n = 0; n < 5; n++) {
      u32 o[2][2];
#pragma unroll
      for (int r = 0; r < 2; r++) {
        o[r][0] = pack_bf16(acc[r][n][0] + bf_lo(xr[r][n][0]), acc[r][n][1] + bf_hi(xr[r][n][0]));
        o[r][1] = pack_bf16(acc[r][n][2] + bf_lo(xr[r][n][1]), acc[r][n][3] + bf_hi(xr[r][n][1]));
      }
      const auto s0 = __builtin_amdgcn_permlane16_swap(o[0][0], o[1][0], false, false);
      const auto s1 = __builtin_amdgcn_permlane16_swap(o[0][1], o[1][1], false, false);
      *(u32x4*)(po + (size_t)n * 131072) = u32x4{s0[0], s1[0], s0[1], s1[1]};
    }
  }
  __builtin_amdgcn_raw_buffer_store_b32(tag, rsC, (bid | threadIdx.x) == 0 ? 0u : OOB, 0, SC1);
  STAMP(25)
  STAMP_OUT(w)
}

extern "C" void kernel_launch(void* const* d_in, const int* in_sizes, int n_in, void* d_out, int out_size,
                              void* d_ws, size_t ws_size, hipStream_t stream) {
  k_all<<<256, 256, 0, stream>>>((const char*)d_in[0], (const char*)d_in[1], (const u32*)d_in[2], (const u32*)d_in[3],
                                 (const char*)d_in[4], (const char*)d_in[5], (const char*)d_in[6], (const char*)d_in[7],
                                 (const char*)d_in[8], (char*)d_ws, (char*)d_out);
}
```

```cpp
#include <hip/hip_runtime.h>
#include <hip/hip_bf16.h>

typedef unsigned u32;
typedef unsigned long long u64;
typedef unsigned short u16;
typedef __attribute__((ext_vector_type(4))) unsigned u32x4;
typedef __attribute__((ext_vector_type(2))) unsigned u32x2;
typedef __attribute__((ext_vector_type(4))) float f32x4;
typedef __attribute__((ext_vector_type(2))) float f32x2;
typedef __attribute__((ext_vector_type(8))) __bf16 bf16x8;
typedef __attribute__((ext_vector_type(2))) __bf16 bf16x2;
#define GAS __attribute__((address_space(1)))
typedef GAS u32 gu32;
typedef GAS u64 gu64;
#define RLX_AGENT __ATOMIC_RELAXED, __HIP_MEMORY_SCOPE_AGENT

constexpr u32 ACT_BYTES = 5242880;
constexpr u32 WP_BYTES = 1884160;
constexpr u32 OFF_ACT1 = 0;
constexpr u32 OFF_ACT2 = ACT_BYTES;
constexpr u32 OFF_WP = 2 * ACT_BYTES;
constexpr u32 OFF_EMB = OFF_WP + 2 * WP_BYTES;
constexpr u32 OFF_CTL = OFF_EMB + 40960;
constexpr u32 C_G1 = 1024, C_G2 = C_G1 + 16384, C_AUX = C_G2 + 16384, C_AUX2 = C_AUX + 16384, CTL_BYTES = C_AUX2 + 16384;
constexpr float LOG2E = 1.4426950408889634f;
constexpr float EPS = 1e-5f;
constexpr u32 SPIN_CAP = 1u << 16;

__device__ __forceinline__ float bf_lo(u32 v) { return __builtin_bit_cast(float, v << 16); }
__device__ __forceinline__ float bf_hi(u32 v) { return __builtin_bit_cast(float, v & 0xffff0000u); }
__device__ __forceinline__ u32 pack_bf16(float lo, float hi) {
  f32x2 v = {lo, hi};
  return __builtin_bit_cast(u32, __builtin_convertvector(v, bf16x2));
}
__device__ __forceinline__ float silu_l2(float yl) {
  float e = __builtin_amdgcn_exp2f(-yl);
  return yl * __builtin_amdgcn_rcpf(__builtin_fmaf(e, LOG2E, LOG2E));
}
template <int CTRL, int RM>
__device__ __forceinline__ float dpp_add(float v) {
  int t = __builtin_amdgcn_update_dpp(0, __builtin_bit_cast(int, v), CTRL, RM, 0xf, true);
  return v + __builtin_bit_cast(float, t);
}
__device__ __forceinline__ float rdlane(float v, int l) {
  return __builtin_bit_cast(float, __builtin_amdgcn_readlane(__builtin_bit_cast(int, v), l));
}
__device__ __forceinline__ float wave_sum(float v) {
  v = dpp_add<0x111, 0xf>(v); v = dpp_add<0x112, 0xf>(v); v = dpp_add<0x114, 0xf>(v); v = dpp_add<0x118, 0xf>(v);
  v = dpp_add<0x142, 0xa>(v); v = dpp_add<0x143, 0xc>(v);
  return rdlane(v, 63);
}
__device__ __forceinline__ float row0_sum(float v) {
  v = dpp_add<0x111, 0xf>(v); v = dpp_add<0x112, 0xf>(v); v = dpp_add<0x114, 0xf>(v); v = dpp_add<0x118, 0xf>(v);
  return rdlane(v, 15);
}
__device__ __forceinline__ float row_allsum(float v) {
  v = dpp_add<0x128, 0xf>(v); v = dpp_add<0x124, 0xf>(v); v = dpp_add<0x122, 0xf>(v); v = dpp_add<0x121, 0xf>(v);
  return v;
}


typedef __amdgpu_buffer_rsrc_t rsrc_t;
__device__ __forceinline__ rsrc_t mk_rsrc(const void* p, u32 bytes) {
  return __builtin_amdgcn_make_buffer_rsrc((void*)p, 0, bytes, 0x00020000);
}
#define SC1 16
#define BLD(rs, vo, so, aux) __builtin_amdgcn_raw_buffer_load_b128(rs, vo, so, aux)
#define SB0 __builtin_amdgcn_sched_barrier(0);
#define BST(v, rs, vo, so) { __builtin_amdgcn_raw_buffer_store_b128(v, rs, vo, so, SC1); SB0 asm volatile("s_nop 1"); SB0 }
#define CFENCE asm volatile("" ::: "memory");
#define DRAIN asm volatile("s_waitcnt vmcnt(0)" ::: "memory");
#define WG_BARRIER { asm volatile("s_waitcnt lgkmcnt(0)" ::: "memory"); __builtin_amdgcn_s_barrier(); asm volatile("" ::: "memory"); }
constexpr u32 OOB = 0x80000000u;
constexpr u32 HOLE = 0xffffffffu;

__device__ __forceinline__ u32 pack_ok(float lo, float hi) { return min(pack_bf16(lo, hi), HOLE - 1u); }
__device__ __forceinline__ u32 umax3(u32 a, u32 b, u32 c) { return max(max(a, b), c); }
#define PIN(v) asm volatile("" : "+v"(v));
__device__ __forceinline__ void group_stats(u32x4 v, float& gmean, float& rstd) {
  const u32 um = v[0], uq = v[2];
  const float m0 = __builtin_bit_cast(float, __builtin_amdgcn_readfirstlane(um));
  float dd = __builtin_bit_cast(float, um) - m0;
  float qq = __builtin_fmaf(10240.f * dd, dd, __builtin_bit_cast(float, uq));
  dd = dpp_add<0x111, 0xf>(dd); qq = dpp_add<0x111, 0xf>(qq);
  dd = dpp_add<0x112, 0xf>(dd); qq = dpp_add<0x112, 0xf>(qq);
  dd = dpp_add<0x114, 0xf>(dd); qq = dpp_add<0x114, 0xf>(qq);
  dd = dpp_add<0x118, 0xf>(dd); qq = dpp_add<0x118, 0xf>(qq);
  const float ds = (rdlane(dd, 15) + rdlane(dd, 31)) * (1.f / 32.f);
  const float var = fmaxf(__builtin_fmaf((rdlane(qq, 15) + rdlane(qq, 31)), 1.f / 327680.f, -ds * ds), 0.f) + EPS;
  gmean = m0 + ds;
  const float r0 = __builtin_amdgcn_rsqf(var);
  rstd = __builtin_fmaf(0.5f * r0, __builtin_fmaf(-var * r0, r0, 1.f), r0);
}
__device__ __forceinline__ int chan1(int n, int i) {
  return n < 4 ? 32 * (n >> 1) + 8 * (i >> 2) + 4 * (n & 1) + (i & 3) : 64 + i;
}

template <int I> struct IC { static constexpr int value = I; };
template <int I, int N, class F>
__device__ __forceinline__ void static_for_(F&& f) {
  if constexpr (I < N) { f(IC<I>{}); static_for_<I + 1, N>(f); }
}
template <int N, class F>
__device__ __forceinline__ void static_for(F&& f) { static_for_<0, N>(f); }

constexpr int REG = 30720;
__host__ __device__ constexpr int a_off(int P) { return (P / 9) * 6144 + ((P % 9) / 3) * 288 + (P % 3) * 16; }
__host__ __device__ constexpr int a_dk(int s) { return s == 22 ? 0 : ((2 * s) % 9 == 8 ? 2 : ((2 * s) % 3 == 2 ? 1 : 0)); }

__global__ __launch_bounds__(256, 1) void k_all(const char* __restrict__ x, const char* __restrict__ temb,
                                                const u32* __restrict__ g1, const u32* __restrict__ be1,
                                                const char* __restrict__ w1, const char* __restrict__ wemb,
                                                const char* __restrict__ g2, const char* __restrict__ be2,
                                                const char* __restrict__ w2, char* ws, char* __restrict__ out) {
  __shared__ __attribute__((aligned(16))) char lds[4 * REG + 256];
  float* const sm = (float*)(lds + 4 * REG);
  const int lane = threadIdx.x & 63;
  const int w = __builtin_amdgcn_readfirstlane(threadIdx.x >> 6);
  const int bid = blockIdx.x;
  const int xb = bid & 7, idx = bid >> 3;
  const int b = xb >> 2, band = xb & 3, og = idx & 3, tile = idx >> 2;
  const int member = band * 8 + tile;
  const int y0 = band * 16 + (tile >> 2) * 8, x0 = (tile & 3) * 16;
  const int li = lane & 15, lj = lane >> 4;
  asm volatile("" :: "s"(x), "s"(temb), "s"(g1), "s"(be1), "s"(w1), "s"(wemb), "s"(g2), "s"(be2), "s"(w2), "s"(ws), "s"(out));
  const u32x4 holes = {HOLE, HOLE, HOLE, HOLE};
  const rsrc_t rsC = mk_rsrc(ws + OFF_CTL, CTL_BYTES);
  u32 tag;
  u32x4 tv[9];
  u32 wdst, wdst2, wq0;
  u32x4 wf;
#define REPACK(tp, rs, dst)                                                                          \
  {                                                                                                  \
    u32x4 f_;                                                                                        \
    _Pragma("unroll") for (int dd = 0; dd < 4; dd++) {                                               \
      const int e0 = 18 * dd + (tp), e1 = e0 + 9;                                                    \
      const u32 sel = ((e0 & 1) ? 0x0302u : 0x0100u) | ((e1 & 1) ? 0x07060000u : 0x05040000u);      \
      f_[dd] = __builtin_amdgcn_perm(tv[e1 >> 3][(e1 >> 1) & 3], tv[e0 >> 3][(e0 >> 1) & 3], sel);   \
    }                                                                                                \
    const u32 qq_ = wq0 + 2 * (tp);                                                                  \
    BST(f_, rs, (dst) + (qq_ >> 2) * 5120 + (qq_ & 3) * 256, 0)                                      \
  }
  const u32 fo = ((bid >> 3) & 15) * 1024 + w * 256;

  auto exchange = [&](float s, float q, float K, u32 coff, auto mid) __attribute__((always_inline)) {
    s = dpp_add<0x111, 0xf>(s); q = dpp_add<0x111, 0xf>(q);
    s = dpp_add<0x112, 0xf>(s); q = dpp_add<0x112, 0xf>(q);
    s = dpp_add<0x114, 0xf>(s); q = dpp_add<0x114, 0xf>(q);
    s = dpp_add<0x118, 0xf>(s); q = dpp_add<0x118, 0xf>(q);
    if (li == 15) *(f32x2*)(sm + 2 * (w * 4 + lj)) = f32x2{K + s * (1.f / 640.f), q - s * s * (1.f / 640.f)};
    WG_BARRIER
    const u32 so = coff + (b * 4 + og) * 512;
    if (w == 0) {
      const f32x2 p = *(const f32x2*)(sm + 2 * li);
      const float m0 = rdlane(p[0], 0);
      float dd = p[0] - m0, qq = __builtin_fmaf(640.f * dd, dd, p[1]);
      dd = dpp_add<0x111, 0xf>(dd); qq = dpp_add<0x111, 0xf>(qq);
      dd = dpp_add<0x112, 0xf>(dd); qq = dpp_add<0x112, 0xf>(qq);
      dd = dpp_add<0x114, 0xf>(dd); qq = dpp_add<0x114, 0xf>(qq);
      dd = dpp_add<0x118, 0xf>(dd); qq = dpp_add<0x118, 0xf>(qq);
      const float mean = __builtin_fmaf(dd, 1.f / 16.f, m0), M2 = __builtin_fmaf(-40.f * dd, dd, qq);
      const u32x4 gr = {__builtin_bit_cast(u32, mean), tag, __builtin_bit_cast(u32, M2), tag};
      BST(gr, rsC, li == 15 ? member * 16 + lj * 4096 : OOB, so);
    }
    SB0
    mid();
    SB0
    if (w == 0) {
      u32x4 pv;
      u32 spins = 0;
      do {
        pv = BLD(rsC, (lane & 31) * 16, so + (member & 3) * 4096, SC1);
        CFENCE
      } while (__builtin_amdgcn_ballot_w64(pv[1] != tag || pv[3] != tag) != 0 && ++spins < SPIN_CAP);
      float gm, rs;
      group_stats(pv, gm, rs);
      *(f32x2*)(sm + 32) = f32x2{gm, rs};
    }
    WG_BARRIER
  };

  {
    const int yp = band * 16 + tile * 2 + (w & 1), c80 = og * 10 + (w >> 1);
    const rsrc_t rsX = mk_rsrc(x + (size_t)(b * 320 + c80 * 8) * 8192 + yp * 128, 72 * 8192);
    const u32 vx = (lane & 1) * 32768 + (lane >> 1) * 4;
    u32 v[5][4];
#pragma unroll
    for (int c = 0; c < 5; c++)
#pragma unroll
      for (int e = 0; e < 4; e++) v[c][e] = __builtin_amdgcn_raw_buffer_load_b32(rsX, vx, (c * 16 + e) * 8192, 0);
    const u32 epoch = __builtin_amdgcn_raw_buffer_load_b32(rsC, 0, 0, SC1);
    SB0
    u32x4 gw[5], bw[5];
    u32 vz = 0u;
    PIN(vz)
#pragma unroll
    for (int c = 0; c < 5; c++) { gw[c] = BLD(mk_rsrc(g1, 640), vz + c * 32, c80 * 16, 0); bw[c] = BLD(mk_rsrc(be1, 640), vz + c * 32, c80 * 16, 0); }
    CFENCE
    SB0
    const rsrc_t rsO = mk_rsrc(ws + OFF_ACT1 + (u32)(b * 40 + c80) * 65536u + yp * 1024, 9 * 65536);
#pragma unroll
    for (int c = 0; c < 5; c++) BST(holes, rsO, lane * 16, c * 131072)
    SB0
    const int j = b * 32 + member;
    const int r2 = (j - 50) * 3 + w - 1;
    const int cv = w == 2, kind = j < 50 ? (w < 2 ? 2 : 0) : (w > 0 && r2 < 40 ? 1 : 2);
    const int u = 4 * j + lj, nt = u / 10, cl = u - nt * 10, tg = nt / 5, n = nt - tg * 5;
    const int r = kind == 1 ? r2 >> 1 : 0, bt = r2 & 1, sl = og * 4 + r / 5, cg = r % 5;
    const rsrc_t rsE = mk_rsrc(temb, 4608);
    const u32 tw0 = __builtin_amdgcn_raw_buffer_load_b32(rsE, min(lane, 35) * 4, sl * 144 + bt * 2304, 0);
    const rsrc_t rsT = mk_rsrc(kind == 0 ? (cv ? w2 : w1) : wemb, kind == 0 ? 1843200 : 737280);
    const u32 vt = kind == 0 ? (u32)(tg * 80 + (cv ? n * 16 + li : chan1(n, li))) * 5760u + (og * 10 + cl) * 144
                             : (kind == 1 ? (u32)(sl * 72 + (lane >> 3)) * 640u + cg * 128 + (lane & 7) * 16 : OOB);
    const int tstep = kind == 0 ? 16 : 5120;
#pragma unroll
    for (int k = 0; k < 9; k++) tv[k] = BLD(rsT, vt, k * tstep, 0);
    SB0
    float ga[5][8], be[5][8];
    const u32 selA = (lane & 1) ? 0x03020c0cu : 0x05040c0cu, selB = (lane & 1) ? 0x07060c0cu : 0x01000c0cu;
    const float K = __builtin_bit_cast(float, __builtin_amdgcn_readfirstlane(v[0][0] << 16));
    float d[5][8];
    float s = 0.f, q = 0.f;
#pragma unroll
    for (int c = 0; c < 5; c++)
#pragma unroll
      for (int e = 0; e < 4; e++) {
        const u32 o = __builtin_amdgcn_update_dpp(0, (int)v[c][e], 0xb1, 0xf, 0xf, true);
        d[c][e] = __builtin_bit_cast(float, __builtin_amdgcn_perm(v[c][e], o, selA)) - K;
        d[c][4 + e] = __builtin_bit_cast(float, __builtin_amdgcn_perm(v[c][e], o, selB)) - K;
        s += d[c][e]; q = __builtin_fmaf(d[c][e], d[c][e], q);
        s += d[c][4 + e]; q = __builtin_fmaf(d[c][4 + e], d[c][4 + e], q);
      }
    tag = __builtin_amdgcn_readfirstlane(epoch) + 0x9E3779B9u;
    SB0
    asm volatile("s_waitcnt vmcnt(10)" ::: "memory");
    wq0 = 18 * (cl >> 1) + (cl & 1);
    wdst = ((tg * 4 + og) * 115 + n) * 1024 + li * 16;
    auto embed = [&]() __attribute__((always_inline)) {
      float a0[8];
#pragma unroll
      for (int c = 0; c < 8; c++) a0[c] = 0.f;
#pragma unroll
      for (int k = 0; k < 9; k++) {
        const u32 t0 = __builtin_amdgcn_ds_bpermute((4 * k + (lane >> 4)) * 4, tw0);
        const float s0 = silu_l2(((lane & 8) ? bf_hi(t0) : bf_lo(t0)) * LOG2E);
#pragma unroll
        for (int c = 0; c < 8; c++) a0[c] = __builtin_fmaf(s0, (c & 1) ? bf_hi(tv[k][c >> 1]) : bf_lo(tv[k][c >> 1]), a0[c]);
      }
      const bool hi = lane & 8;
      f32x4 o;
#pragma unroll
      for (int c = 0; c < 8; c++) a0[c] = dpp_add<0x128, 0xf>(a0[c]);
#pragma unroll
      for (int c = 0; c < 4; c++) {
        float l_ = a0[c], h_ = a0[4 + c];
        PIN(l_) PIN(h_)
        float t_ = hi ? h_ : l_;
        t_ += __shfl_xor(t_, 16);
        o[c] = t_ + __shfl_xor(t_, 32);
      }
      BST(__builtin_bit_cast(u32x4, o), mk_rsrc(ws + OFF_EMB, 40960),
          lane < 16 ? (u32)((bt * 16 + sl) * 320 + cg * 64 + (lane & 7) * 8 + (hi ? 4 : 0)) * 4u : OOB, 0)
    };
    exchange(s, q, K, C_G1, [&]() __attribute__((always_inline)) {
      if (kind == 0 && w == 3) {
#pragma unroll
        for (int tp = 0; tp < 9; tp++) REPACK(tp, mk_rsrc(ws + OFF_WP, WP_BYTES), wdst)
      }
      if (w == 3 && kind != 0)
        __builtin_amdgcn_raw_buffer_store_b32(tag, rsC, lane < 16 ? lane * 1024 : OOB, C_AUX + og * 256 + j * 4, SC1);
      if (kind == 1) embed();
#pragma unroll
    for (int c = 0; c < 5; c++)
#pragma unroll
      for (int e = 0; e < 4; e++) {
        const u32 gw_ = gw[c][e], bw_ = bw[c][e];
        ga[c][2 * e] = bf_lo(gw_) * LOG2E; ga[c][2 * e + 1] = bf_hi(gw_) * LOG2E;
        be[c][2 * e] = bf_lo(bw_) * LOG2E; be[c][2 * e + 1] = bf_hi(bw_) * LOG2E;
        PIN(ga[c][2 * e]) PIN(ga[c][2 * e + 1]) PIN(be[c][2 * e]) PIN(be[c][2 * e + 1])
      }
#pragma unroll
      for (int c = 0; c < 5; c++)
#pragma unroll
        for (int e = 0; e < 8; e++) { d[c][e] *= ga[c][e]; PIN(d[c][e]) }
      if (w == 3 && kind == 0) {
        DRAIN
        __builtin_amdgcn_raw_buffer_store_b32(tag, rsC, lane < 16 ? lane * 1024 : OOB, C_AUX + og * 256 + j * 4, SC1);
      }
    });
    u32 p2 = __builtin_amdgcn_raw_buffer_load_b32(rsC, lane * 4, C_AUX + fo, SC1), p3;
    SB0
    const f32x2 gs = *(const f32x2*)(sm + 32);
    const float rs = gs[1], mr = (gs[0] - K) * rs;
#pragma unroll
    for (int c = 0; c < 5; c++) {
      float o[8];
#pragma unroll
      for (int e = 0; e < 8; e++) o[e] = silu_l2(__builtin_fmaf(d[c][e], rs, __builtin_fmaf(-mr, ga[c][e], be[c][e])));
      const u32x4 p = {pack_ok(o[0], o[1]), pack_ok(o[2], o[3]), pack_ok(o[4], o[5]), pack_ok(o[6], o[7])};
      BST(p, rsO, lane * 16, c * 131072);
      if (c == 1) p3 = __builtin_amdgcn_raw_buffer_load_b32(rsC, lane * 4, C_AUX + fo, SC1);
      SB0
    }
    {
      const bool mine = kind == 0 && w == 2;
      const u32 q1 = wq0 + 2;
      wdst2 = mine ? wdst + (q1 >> 2) * 5120 + (q1 & 3) * 256 : OOB;
      wdst = mine ? wdst + (wq0 >> 2) * 5120 + (wq0 & 3) * 256 : OOB;
      PIN(wdst) PIN(wdst2)
    }
    if (__builtin_amdgcn_ballot_w64(p2 != tag) != 0) {
      for (u32 spins = 0; __builtin_amdgcn_ballot_w64(p3 != tag) != 0 && spins < SPIN_CAP; ++spins) {
        p3 = __builtin_amdgcn_raw_buffer_load_b32(rsC, lane * 4, C_AUX + fo, SC1);
        CFENCE
      }
    }
    CFENCE
  }

  u32 voffA[3];
#pragma unroll
  for (int i = 0; i < 3; i++) {
    const int p = i * 64 + lane, row = p / 18, px = p - row * 18;
    const int yy = y0 - 1 + row, xx = x0 - 1 + px;
    const bool ok = p < 180 && (u32)yy < 64u && (u32)xx < 64u;
    voffA[i] = ok ? (u32)(yy * 64 + xx) * 16u : OOB;
  }
  u32 voffB4 = lane * 16 + 4096;
  asm volatile("" : "+v"(voffB4));
  const u32 voffB = lane * 16, voffBl = lane < 32 ? lane * 16 : OOB;
  char* const ldsw = lds + w * REG + lane * 16;
  const char* abase[4][3];
  {
    const char* lb = lds + w * REG + (lj & 1) * 3072 + li * 16;
    const bool hi = lj >> 1;
#pragma unroll
    for (int p = 0; p < 4; p++) {
      const char* r = lb + ((p + w) & 3) * 576;
      abase[p][0] = r + (hi ? 16 : 0); abase[p][1] = r + (hi ? 256 : 0); abase[p][2] = r + (hi ? 5536 : 0);
    }
  }
  const rsrc_t rsB1 = mk_rsrc(ws + OFF_WP + (og * 4 + w) * 117760, 117760);
  const rsrc_t rsB2 = mk_rsrc(ws + OFF_WP + WP_BYTES + (og * 4 + w) * 117760, 117760);
  const rsrc_t rsO2 = mk_rsrc(ws + OFF_ACT2 + (u32)(b * 40 + og * 10) * 65536u + ((y0 + 2 * w) * 64 + x0) * 16, 10 * 65536);
  const u32 vo2 = lj * 65536 + li * 16, vo2l = (8 + (lj >> 1)) * 65536 + (lj & 1) * 1024 + li * 16;

  u32x4 st[2][6];
  u32x4 bf[4][5];
  u32x4 af[2][8];
  f32x4 acc[8][5];
  f32x4 em[5];
  u32x4 gq[2], bq[2];
  u32x2 g4, b4;
  u32x2 xr[2][5];
  u32 a2;
#define LOAD_A1(c, slot, i_) st[slot][i_] = BLD(rsA, voffA[(i_) % 3], ((c) * 2 + (i_) / 3) * 65536, SC1);
#define LOAD_A(c, slot) _Pragma("unroll") for (int i_ = 0; i_ < 6; i_++) { LOAD_A1(c, slot, i_) }
#define MAX4(mx, v) mx = umax3(umax3(mx, (v)[0], (v)[1]), (v)[2], (v)[3]);
#define CHECK_A(c)                                                                                \
  if (__builtin_expect(__builtin_amdgcn_ballot_w64(hm == HOLE) != 0, 0)) {                        \
    u32x4 t_[6];                                                                                  \
    u32 spins_ = 0;                                                                               \
    do {                                                                                          \
      _Pragma("unroll") for (int i_ = 0; i_ < 6; i_++) t_[i_] = BLD(rsA, voffA[i_ % 3], ((c) * 2 + i_ / 3) * 65536, SC1); \
      CFENCE                                                                                      \
      hm = 0u;                                                                                    \
      _Pragma("unroll") for (int i_ = 0; i_ < 6; i_++) { MAX4(hm, t_[i_]) }                       \
    } while (__builtin_amdgcn_ballot_w64(hm == HOLE) != 0 && ++spins_ < SPIN_CAP);                \
    _Pragma("unroll") for (int i_ = 0; i_ < 6; i_++) *(u32x4*)(ldsw + (c) * 6144 + i_ * 1024) = t_[i_]; \
  }                                                                                               \
  hm = 0u;
#define WRITE_A1(c, slot, i_) { *(u32x4*)(ldsw + (c) * 6144 + (i_) * 1024) = st[slot][i_]; MAX4(hm, st[slot][i_]) }
#define LOAD_B(rs, s, n) bf[(s) & 3][n] = (s) == 22 ? BLD(rs, voffBl, ((s) * 5 + (n)) * 1024, SC1) : BLD(rs, (n) < 4 ? voffB + (n) * 1024 : voffB4, (s) * 5120, SC1);
#define MFMA_(first, c, wt, px, WC)                                                                 \
  if (CONV == 1) {                                                                                  \
    if (first) asm volatile("v_mfma_f32_16x16x32_bf16 %0, %1, %2, 0" : "=&a"(c) : WC(wt), "v"(px));  \
    else asm volatile("v_mfma_f32_16x16x32_bf16 %0, %1, %2, %0" : "+a"(c) : WC(wt), "v"(px));        \
  } else {                                                                                          \
    if (first) asm volatile("v_mfma_f32_16x16x32_bf16 %0, %2, %1, 0" : "=&a"(c) : WC(wt), "v"(px));  \
    else asm volatile("v_mfma_f32_16x16x32_bf16 %0, %2, %1, %0" : "+a"(c) : WC(wt), "v"(px));        \
  }
#define MFMA(first, c, wt, px) if (s & 1) { MFMA_(first, c, wt, px, "a") } else { MFMA_(first, c, wt, px, "v") }
#define READ_A(s, m) af[(s) & 1][m] = *(const u32x4*)(abase[(m) >> 1][a_dk(s)] + a_off(2 * (s)) + ((m) & 1) * 288);

  auto conv = [&](auto cc) __attribute__((always_inline)) {
    constexpr int CONV = decltype(cc)::value;
    const rsrc_t rsA = mk_rsrc(ws + (CONV == 1 ? OFF_ACT1 : OFF_ACT2) + (u32)(b * 40 + w * 10) * 65536u, 10 * 65536);
    const rsrc_t rsB = CONV == 1 ? rsB1 : rsB2;
    if (CONV == 1) {
#pragma unroll
      for (int n = 0; n < 5; n++) { LOAD_B(rsB, 0, n) }
      SB0
    }
    LOAD_A(0, 0)
    SB0
    if (CONV == 1) {
#pragma unroll
      for (int n = 0; n < 5; n++) { LOAD_B(rsB, 1, n) }
#pragma unroll
      for (int n = 0; n < 5; n++) { LOAD_B(rsB, 2, n) }
#pragma unroll
      for (int n = 0; n < 5; n++) { LOAD_B(rsB, 3, n) }
      SB0
    }
    u32 hm = 0u;
#pragma unroll
    for (int i = 0; i < 6; i++) WRITE_A1(0, 0, i)
    SB0
    CHECK_A(0)
#pragma unroll
    for (int m = 0; m < 8; m++) { READ_A(0, m) }
    SB0
    LOAD_A(1, 1)
    SB0

    static_for<23>([&](auto sc) __attribute__((always_inline)) {
      constexpr int s = decltype(sc)::value;
      if (CONV == 1 && s == 9) {
        asm volatile("s_waitcnt vmcnt(20)" ::: "memory");
        __builtin_amdgcn_s_barrier();
        CFENCE
        __builtin_amdgcn_raw_buffer_store_b32(tag, rsC, threadIdx.x < 16 ? threadIdx.x * 1024 : OOB, C_AUX2 + bid * 4, SC1);
      }
      if (CONV == 1 && s == 16) a2 = __builtin_amdgcn_raw_buffer_load_b32(rsC, lane * 4, C_AUX2 + fo, SC1);
      if constexpr (s == 22) {
#pragma unroll
        for (int t = 0; t < 40; t++) {
          const int m = (t / 5 + 2) & 7, n = t % 5;
          MFMA(false, acc[m][n], bf[s & 3][n], af[s & 1][m])
          if (t >= 3 && t < 33) *(f32x4*)(ldsw + (t - 3) * 1024) = acc[(t - 3) / 5 + 2][(t - 3) % 5];
          if (CONV == 1 && t < 30 && (t & 1) == 0) { const int i = t >> 1, s2 = i < 10 ? i / 5 : 3; LOAD_B(rsB2, s2, i % 5) }
          SB0
        }
        return;
      }
#pragma unroll
      for (int n = 0; n < 5; n++) {
#pragma unroll
        for (int m = 0; m < 8; m++) {
          MFMA(s == 0, acc[m][n], bf[s & 3][n], af[s & 1][m])
          if (s < 22 && n < 4 && (m == 1 || m == 5)) { READ_A(s + 1, 2 * n + (m >> 2)) SB0 }
          if (CONV == 1 && s >= 1 && s <= 3 && n < 3) {
            const int tp = (s - 1) * 3 + n;
            if (m == 2 || m == 3) {
#pragma unroll
              for (int dd = 2 * m - 4; dd < 2 * m - 2; dd++) {
                const int e0 = 18 * dd + tp, e1 = e0 + 9;
                const u32 sel = ((e0 & 1) ? 0x0302u : 0x0100u) | ((e1 & 1) ? 0x07060000u : 0x05040000u);
                u32 f_ = __builtin_amdgcn_perm(tv[e1 >> 3][(e1 >> 1) & 3], tv[e0 >> 3][(e0 >> 1) & 3], sel);
                PIN(f_)
                wf[dd] = f_;
              }
              SB0
            }
            if (m == 6) BST(wf, mk_rsrc(ws + OFF_WP + WP_BYTES, WP_BYTES), (tp & 1) ? wdst2 : wdst, (tp >> 1) * 5120)
          }
          if (n == 4 && m < 6) {
            if (s == 2) WRITE_A1(1, 1, m)
            if (s == 6) WRITE_A1(2, 0, m)
            if (s == 10) WRITE_A1(3, 1, m)
            if (s == 15) WRITE_A1(4, 0, m)
            if (s == 0) { LOAD_A1(2, 0, m) }
            if (s == 3) { LOAD_A1(3, 1, m) }
            if (s == 7) { LOAD_A1(4, 0, m) }
            if (CONV == 1 && m < 5) {
              if (s == 4) BST(holes, rsO2, m < 4 ? vo2 : vo2l, m < 4 ? (m & 1) * 1024 + (m >> 1) * 262144 : 0)
              if (s == 19) em[m] = __builtin_bit_cast(f32x4, BLD(mk_rsrc(ws + OFF_EMB, 40960), ((b * 16 + li) * 320 + og * 80 + chan1(m, 4 * lj)) * 4, 0, SC1));
              if (s == 20) {
                if (m < 2) gq[m] = *(const u32x4*)(g2 + (og * 80 + 32 * m + 8 * lj) * 2);
                else if (m < 4) bq[m - 2] = *(const u32x4*)(be2 + (og * 80 + 32 * (m - 2) + 8 * lj) * 2);
                else { g4 = *(const u32x2*)(g2 + (og * 80 + 64 + 4 * lj) * 2); b4 = *(const u32x2*)(be2 + (og * 80 + 64 + 4 * lj) * 2); }
              }
            }
            if (CONV == 2 && (s == 18 || s == 19) && m < 5) {
              const int r = s & 1;
              xr[r][m] = *(const u32x2*)(x + ((size_t)(b * 320 + og * 80 + m * 16 + li) * 4096 + (y0 + 2 * w + r) * 64 + x0 + 4 * lj) * 2);
            }
            SB0
          }
          if (CONV == 1 && s == 21 && m >= 4) { float e_ = row_allsum(em[n][m - 4]); asm volatile("" : "+v"(e_)); em[n][m - 4] = e_; }
        }
        if (s + 4 < 23) { LOAD_B(rsB, s + 4, n) }
        SB0
      }
      if (s == 2) { CHECK_A(1) }
      if (s == 6) { CHECK_A(2) }
      if (s == 10) { CHECK_A(3) }
      if (s == 15) { CHECK_A(4) }
      if (CONV == 1 && s == 18) {
        if (__builtin_expect(__builtin_amdgcn_ballot_w64(a2 != tag) != 0, 0)) {
          u32 spins = 0;
          do {
            a2 = __builtin_amdgcn_raw_buffer_load_b32(rsC, lane * 4, C_AUX2 + fo, SC1);
            CFENCE
          } while (__builtin_amdgcn_ballot_w64(a2 != tag) != 0 && ++spins < SPIN_CAP);
        }
        CFENCE
        __builtin_amdgcn_s_barrier();
        CFENCE
      }
    });
    asm volatile("s_nop 15\n s_nop 7"
                 : "+a"(acc[0][0]), "+a"(acc[0][1]), "+a"(acc[0][2]), "+a"(acc[0][3]), "+a"(acc[0][4]),
                   "+a"(acc[1][0]), "+a"(acc[1][1]), "+a"(acc[1][2]), "+a"(acc[1][3]), "+a"(acc[1][4])
                 :: "memory");
    SB0
    if (CONV == 1) {
#pragma unroll
      for (int n = 0; n < 5; n++) { LOAD_B(rsB2, 2, n) }
      SB0
    }
    WG_BARRIER
    {
      f32x4 t[3][10];
#define RED_READ(d) _Pragma("unroll") for (int i = 0; i < 10; i++)                                 \
      t[(d) - 1][i] = *(const f32x4*)(lds + ((w - (d)) & 3) * REG + ((d) - 1) * 10240 + lane * 16 + i * 1024);
#define RED_ADD(d) _Pragma("unroll") for (int i = 0; i < 10; i++) acc[i / 5][i % 5] += t[(d) - 1][i];
      RED_READ(1) RED_READ(2) SB0
      RED_ADD(1) SB0
      RED_READ(3) SB0
      RED_ADD(2) RED_ADD(3)
    }
  };

  conv(IC<1>{});
  {
    const float K = __builtin_bit_cast(float, __builtin_amdgcn_readfirstlane(__builtin_bit_cast(int, acc[0][0][0] + em[0][0])));
    float s = 0.f, q = 0.f;
#pragma unroll
    for (int n = 0; n < 5; n++)
#pragma unroll
      for (int k = 0; k < 4; k++) {
        const float ek = em[n][k] - K;
#pragma unroll
        for (int r = 0; r < 2; r++) {
          const float dv = acc[r][n][k] + ek;
          acc[r][n][k] = dv; s += dv; q = __builtin_fmaf(dv, dv, q);
        }
      }
    float ga[5][4], be[5][4];
    exchange(s, q, K, C_G2, [&]() __attribute__((always_inline)) {
#pragma unroll
      for (int n = 0; n < 5; n++)
#pragma unroll
        for (int k = 0; k < 4; k++) {
          const u32 gw = n < 4 ? gq[n >> 1][(n & 1) * 2 + (k >> 1)] : g4[k >> 1];
          const u32 bw = n < 4 ? bq[n >> 1][(n & 1) * 2 + (k >> 1)] : b4[k >> 1];
          ga[n][k] = ((k & 1) ? bf_hi(gw) : bf_lo(gw)) * LOG2E;
          be[n][k] = ((k & 1) ? bf_hi(bw) : bf_lo(bw)) * LOG2E;
          float t0 = acc[0][n][k] * ga[n][k], t1 = acc[1][n][k] * ga[n][k];
          PIN(ga[n][k]) PIN(be[n][k]) PIN(t0) PIN(t1)
          acc[0][n][k] = t0; acc[1][n][k] = t1;
        }
    });
    const f32x2 gs = *(const f32x2*)(sm + 32);
    const float rs = gs[1], mr = (gs[0] - K) * rs;
    u32 pk[2][5][2];
#pragma unroll
    for (int n = 0; n < 5; n++) {
      float o[2][4];
#pragma unroll
      for (int k = 0; k < 4; k++) {
        const float c = __builtin_fmaf(-mr, ga[n][k], be[n][k]);
        o[0][k] = silu_l2(__builtin_fmaf(acc[0][n][k], rs, c));
        o[1][k] = silu_l2(__builtin_fmaf(acc[1][n][k], rs, c));
      }
#pragma unroll
      for (int r = 0; r < 2; r++) { pk[r][n][0] = pack_ok(o[r][0], o[r][1]); pk[r][n][1] = pack_ok(o[r][2], o[r][3]); }
      if (n & 1) {
#pragma unroll
        for (int r = 0; r < 2; r++) {
          const u32x4 p = {pk[r][n - 1][0], pk[r][n - 1][1], pk[r][n][0], pk[r][n][1]};
          BST(p, rsO2, vo2, r * 1024 + (n >> 1) * 262144);
        }
        SB0
      }
    }
    {
      const auto s0 = __builtin_amdgcn_permlane16_swap(pk[0][4][0], pk[1][4][0], false, false);
      const auto s1 = __builtin_amdgcn_permlane16_swap(pk[0][4][1], pk[1][4][1], false, false);
      const u32x4 p = {s0[0], s1[0], s0[1], s1[1]};
      BST(p, rsO2, vo2l, 0);
      SB0
    }
    CFENCE
  }
  conv(IC<2>{});
  {
    char* const po = out + ((size_t)(b * 320 + og * 80 + li) * 4096 + (y0 + 2 * w + (lj & 1)) * 64 + x0 + 8 * (lj >> 1)) * 2;
#pragma unroll
    for (int n = 0; n < 5; n++) {
      u32 o[2][2];
#pragma unroll
      for (int r = 0; r < 2; r++) {
        o[r][0] = pack_bf16(acc[r][n][0] + bf_lo(xr[r][n][0]), acc[r][n][1] + bf_hi(xr[r][n][0]));
        o[r][1] = pack_bf16(acc[r][n][2] + bf_lo(xr[r][n][1]), acc[r][n][3] + bf_hi(xr[r][n][1]));
      }
      const auto s0 = __builtin_amdgcn_permlane16_swap(o[0][0], o[1][0], false, false);
      const auto s1 = __builtin_amdgcn_permlane16_swap(o[0][1], o[1][1], false, false);
      *(u32x4*)(po + (size_t)n * 131072) = u32x4{s0[0], s1[0], s0[1], s1[1]};
    }
  }
  __builtin_amdgcn_raw_buffer_store_b32(tag, rsC, (bid | threadIdx.x) == 0 ? 0u : OOB, 0, SC1);
}

extern "C" void kernel_launch(void* const* d_in, const int* in_sizes, int n_in, void* d_out, int out_size,
                              void* d_ws, size_t ws_size, hipStream_t stream) {
  k_all<<<256, 256, 0, stream>>>((const char*)d_in[0], (const char*)d_in[1], (const u32*)d_in[2], (const u32*)d_in[3],
                                 (const char*)d_in[4], (const char*)d_in[5], (const char*)d_in[6], (const char*)d_in[7],
                                 (const char*)d_in[8], (char*)d_ws, (char*)d_out);
}
```
